```python
import torch

def _softmax(x, axis=-1):
    return torch.softmax(x, dim=axis)
def _gelu(x):
    return torch.nn.functional.gelu(x, approximate="tanh")
def _silu(x):
    return torch.nn.functional.silu(x)
def _sigmoid(x):
    return torch.sigmoid(x)
def _layernorm(x, axis=-1, eps=1e-5):
    m = x.mean(dim=axis, keepdim=True)
    v = x.var(dim=axis, keepdim=True, unbiased=False)
    return (x - m) / torch.sqrt(v + eps)
def _scatter_add(tgt, idx, src, axis=0):
    return tgt.clone().index_add_(axis, idx, src)
def _segment_sum(src, seg, n):
    out = torch.zeros((n,) + src.shape[1:], dtype=src.dtype)
    return out.index_add_(0, seg, src)
def _pad(x, pw):
    flat = tuple(v for pair in reversed(pw) for v in pair)
    return torch.nn.functional.pad(x, flat)

kernel_name = 'recurrent_scan__ssm_conv_s5__dense_activation_gelu__prompt'

def setup_inputs():

    _w = (lambda a: (lambda t: t.to(torch.bfloat16) if t.is_floating_point() else t)(torch.as_tensor(a)))
    def _A_setup():
        B, S, D = 1, 2048, 640
        P = D
        Lambda_re = -0.5 * (lambda _x: torch.exp(torch.as_tensor(_x)))((lambda *s: torch.randn(*s, dtype=torch.bfloat16) * 0.4)(P))
        Lambda_im = (lambda *s: torch.randn(*s, dtype=torch.bfloat16) * 0.8)(P) * 1.0
        B_re = (lambda *s: torch.randn(*s, dtype=torch.bfloat16) * 0.8)(P, D) * (D ** -0.5)
        B_im = (lambda *s: torch.randn(*s, dtype=torch.bfloat16) * 0.8)(P, D) * (D ** -0.5)
        C_re = (lambda *s: torch.randn(*s, dtype=torch.bfloat16) * 0.4)(D, P) * (P ** -0.5)
        C_im = (lambda *s: torch.randn(*s, dtype=torch.bfloat16) * 0.8)(D, P) * (P ** -0.5)
        log_dt = (lambda *s: torch.randn(*s, dtype=torch.bfloat16) * 0.4)(P) * 0.3 - 2.0
        Dskip = (lambda *s: torch.randn(*s, dtype=torch.bfloat16) * 0.2)(D)
        return {
            "x": _w((lambda *s: torch.randn(*s, dtype=torch.bfloat16) * 0.05)(B, S, D)),
            "Lambda_re": _w(Lambda_re),
            "Lambda_im": _w(Lambda_im),
            "B_re": _w(B_re),
            "B_im": _w(B_im),
            "C_re": _w(C_re),
            "C_im": _w(C_im),
            "log_dt": _w(log_dt),
            "Dskip": _w(Dskip),
        }
    _A_in = _A_setup()

    def _B_setup():
        B, S, D, F = 1, 2048, 640, 1536
        return {
            "x": _w((lambda *s: torch.randn(*s, dtype=torch.bfloat16) * 0.05)(B, S, D)),
            "w1": _w((lambda *s: torch.randn(*s, dtype=torch.bfloat16) * 0.8)(D, F) * (D ** -0.5)),
            "b1": _w((lambda *s: torch.randn(*s, dtype=torch.bfloat16) * 12.8)(F) * 0.01),
            "w2": _w((lambda *s: torch.randn(*s, dtype=torch.bfloat16) * 1.6)(F, D) * (F ** -0.5)),
            "b2": _w((lambda *s: torch.randn(*s, dtype=torch.bfloat16) * 12.8)(D) * 0.01),
        }
    _B_in = _B_setup()

    return {
        "A_x": _A_in["x"],
        "A_Lambda_re": _A_in["Lambda_re"],
        "A_Lambda_im": _A_in["Lambda_im"],
        "A_B_re": _A_in["B_re"],
        "A_B_im": _A_in["B_im"],
        "A_C_re": _A_in["C_re"],
        "A_C_im": _A_in["C_im"],
        "A_log_dt": _A_in["log_dt"],
        "A_Dskip": _A_in["Dskip"],
        "B_x": _B_in["x"],
        "B_w1": _B_in["w1"],
        "B_b1": _B_in["b1"],
        "B_w2": _B_in["w2"],
        "B_b2": _B_in["b2"],
    }

def reference(A_x, A_Lambda_re, A_Lambda_im, A_B_re, A_B_im, A_C_re, A_C_im, A_log_dt, A_Dskip, B_x, B_w1, B_b1, B_w2, B_b2):
    def _A_ref(x, Lambda_re, Lambda_im, B_re, B_im, C_re, C_im, log_dt, Dskip):
        Bsz, L, H = x.shape
        Lam = Lambda_re + 1j * Lambda_im
        Bmat = B_re + 1j * B_im
        Cmat = C_re + 1j * C_im
        dt = torch.exp(log_dt)
        Lam_bar = torch.exp(dt * Lam)
        B_bar = ((Lam_bar - 1.0) / Lam)[:, None] * Bmat
        Bu = torch.einsum("bld,pd->blp", x + 0j, B_bar)
        h = torch.zeros_like(Bu[:, 0, :])
        hs = []
        for t in range(L):
            h = Lam_bar[None, :] * h + Bu[:, t, :]
            hs.append(h)
        hseq = torch.stack(hs, 1)
        y = 2.0 * torch.einsum("blp,dp->bld", hseq, Cmat).real + x * Dskip[None, None, :]
        return y

    def _B_ref(x, w1, b1, w2, b2):
        h = torch.einsum("bsd,df->bsf", x, w1) + b1
        h = _gelu(h)
        return torch.einsum("bsf,fd->bsd", h, w2) + b2

    x = A_x
    xn = _layernorm(x, axis=-1)
    s = _A_ref(xn, A_Lambda_re, A_Lambda_im, A_B_re, A_B_im, A_C_re, A_C_im, A_log_dt, A_Dskip)
    g = _silu(_B_ref(xn, B_w1, B_b1, B_w2, B_b2))
    return x + s * g
```

```cpp
#define ROTV 2
#define OUT_AUX 2
#include <hip/hip_runtime.h>
#include <stdint.h>

typedef unsigned short u16;
typedef unsigned long long u64;
typedef unsigned v4u __attribute__((ext_vector_type(4)));
typedef unsigned v2u __attribute__((ext_vector_type(2)));
typedef float f32x4 __attribute__((ext_vector_type(4)));
typedef float f32x2 __attribute__((ext_vector_type(2)));
typedef short bf16x8 __attribute__((ext_vector_type(8)));
typedef short v4s __attribute__((ext_vector_type(4)));
typedef __bf16 v2bf __attribute__((ext_vector_type(2)));
#define GAS __attribute__((address_space(1)))
#define LAS __attribute__((address_space(3)))
typedef GAS u64 gu64;
#define RLX_AGENT __ATOMIC_RELAXED, __HIP_MEMORY_SCOPE_AGENT
#define RLX_WG __ATOMIC_RELAXED, __HIP_MEMORY_SCOPE_WORKGROUP

constexpr int L_ = 2048, D_ = 640, P_ = 640, F_ = 1536;
constexpr unsigned SPIN_CAP = 1u << 20;
constexpr unsigned WS_E = 0;
constexpr unsigned WS_RDY = 1024;
constexpr unsigned WS_PT = 4096;
constexpr unsigned WS_FH = 8192;
constexpr unsigned WS_FS = 16384;
constexpr unsigned WS_STAT = 32768;
constexpr unsigned WS_AGG = 65536;
constexpr unsigned WS_HF = WS_AGG + 16 * 40 * 16 * 16;
constexpr unsigned WS_HS = WS_HF + 32 * 48 * 4096;
constexpr unsigned WS_W2T = WS_HS + 32 * 40 * 4096;
constexpr unsigned WS_END = WS_W2T + 8 * 48 * 5120;

__device__ __forceinline__ float bf2f(unsigned v16) { return __builtin_bit_cast(float, v16 << 16); }
__device__ __forceinline__ float bflo(unsigned v) { return __builtin_bit_cast(float, v << 16); }
__device__ __forceinline__ float bfhi(unsigned v) { return __builtin_bit_cast(float, v & 0xffff0000u); }
__device__ __forceinline__ unsigned pk2(float a, float b) { v2bf r = {(__bf16)a, (__bf16)b}; return __builtin_bit_cast(unsigned, r); }
__device__ __forceinline__ float asf(unsigned v) { return __builtin_bit_cast(float, v); }
__device__ __forceinline__ unsigned asu(float v) { return __builtin_bit_cast(unsigned, v); }
__device__ __forceinline__ float rbf(float a) { return (float)(__bf16)a; }
template <int CTRL> __device__ __forceinline__ float dpp(float x) {
  return __builtin_bit_cast(float, __builtin_amdgcn_mov_dpp(__builtin_bit_cast(int, x), CTRL, 0xf, 0xf, true));
}
__device__ __forceinline__ float swp(float x) { return dpp<0xB1>(x); }
__device__ __forceinline__ float cm(float mr, float mi, float v) { return fmaf(mr, v, mi * swp(v)); }
__device__ __forceinline__ float bperm(int byteaddr, float v) {
  return __builtin_bit_cast(float, __builtin_amdgcn_ds_bpermute(byteaddr, __builtin_bit_cast(int, v)));
}
__device__ __forceinline__ void rows16(float x, float& ev, float& od) {
  const auto r = __builtin_amdgcn_permlane16_swap(asu(x), asu(x), false, false); ev = asf(r[0]); od = asf(r[1]);
}
__device__ __forceinline__ void rows32(float x, float& lo, float& hi) {
  const auto r = __builtin_amdgcn_permlane32_swap(asu(x), asu(x), false, false); lo = asf(r[0]); hi = asf(r[1]);
}
__device__ __forceinline__ f32x4 mfma(v4u a, v4u b, f32x4 c) {
  return __builtin_amdgcn_mfma_f32_16x16x32_bf16(__builtin_bit_cast(bf16x8, a), __builtin_bit_cast(bf16x8, b), c, 0, 0, 0);
}
#define WAITV(n) asm volatile("s_waitcnt vmcnt(" #n ")" ::: "memory")
#define WAITL() asm volatile("s_waitcnt lgkmcnt(0)" ::: "memory")
#define BAR() do { WAITL(); __builtin_amdgcn_s_barrier(); asm volatile("" ::: "memory"); } while (0)
#define SGB(mask, n) __builtin_amdgcn_sched_group_barrier(mask, n, 0)
#define SCHED() __builtin_amdgcn_sched_barrier(0)
struct cpx { float r, i; };
__device__ __forceinline__ cpx cmul(cpx a, cpx b) { return {a.r * b.r - a.i * b.i, a.r * b.i + a.i * b.r}; }
__device__ __forceinline__ cpx csq(cpx a) { return {a.r * a.r - a.i * a.i, 2.f * a.r * a.i}; }

#define STAMP_DECL
#define STAMP(i)
#define STAMP_OUT()

#define TIMED(i, stmt) { stmt; }
#define SP
#define SA
#define FS_B0 10
#define FH_DELAY 80
#define FH_SLP 8
#define FS_DELAY 16
#define FS_SLP 16
constexpr int XOFF = 0, BRE = 16384, BIM = BRE + 5120 + 128, WOFF = BIM + 5120, STG = WOFF + 12288, MISC = 4 * STG;
constexpr int M_CT = MISC, M_MRS = M_CT + 80 * 48, M_RS = M_MRS + 512, M_CS = M_RS + 512, M_WAGG = M_CS + 704, M_B1 = M_WAGG + 1280,
              M_HIN = M_B1 + 192, M_SYNC = M_HIN + 320, K_LDS = M_SYNC + 96;
static_assert(K_LDS <= 163840, "lds");
constexpr int SY_C1 = M_SYNC, SY_C2 = M_SYNC + 4, SY_ONE = M_SYNC + 8, SY_FULL = M_SYNC + 16, SY_FREE = M_SYNC + 32, SY_TX = M_SYNC + 64,
              SY_T = M_SYNC + 80, SY_C3 = M_SYNC + 88, SY_HOK = M_SYNC + 24, SY_SOK = M_SYNC + 28;
constexpr int SL2 = 20480, M_IMG = 7 * SL2;
#define DA 8
constexpr int NU = 44, DB = 12;
__device__ __forceinline__ constexpr int slotu(int j) { return ((j + 8) % 14) * 10240; }
static_assert(M_IMG + 4 * 3072 <= MISC, "img");

#define DMA16(rsrc, ldsoff, voff, soff, aux) __builtin_amdgcn_raw_ptr_buffer_load_lds(rsrc, (LAS void*)(S + (ldsoff)), 16, voff, soff, 0, aux)

__device__ __forceinline__ bool allw(bool p) { return __builtin_amdgcn_ballot_w64(p) == ~0ull; }
__device__ __forceinline__ void lds_wait_ge(LAS char* S, int off, unsigned want) {
#pragma nounroll
  for (unsigned spins = 0; spins < SPIN_CAP; ++spins) {
    if (allw(__hip_atomic_load((LAS unsigned*)(S + off), RLX_WG) >= want)) break;
    __builtin_amdgcn_s_sleep(1);
  }
  asm volatile("" ::: "memory");
}
__device__ __forceinline__ void lds_wait_sl(LAS char* S, int off, unsigned want) {
#pragma nounroll
  for (unsigned spins = 0; spins < SPIN_CAP; ++spins) {
    if (allw(__hip_atomic_load((LAS unsigned*)(S + off), RLX_WG) >= want)) break;
    __builtin_amdgcn_s_sleep(4);
  }
  asm volatile("" ::: "memory");
}
__device__ __forceinline__ void lds_set(LAS char* S, int off, unsigned v, int lane) {
  if (lane == 0) __hip_atomic_store((LAS unsigned*)(S + off), v, RLX_WG);
}
__device__ __forceinline__ bool ld_test(unsigned addr, unsigned want) {
  unsigned v;
  asm volatile("ds_read_b32 %0, %1\n\ts_waitcnt lgkmcnt(0)" : "=v"(v) : "v"(addr) : "memory");
  return allw(v >= want);
}
__device__ __forceinline__ void ld_wait(unsigned addr, unsigned want) {
#pragma nounroll
  for (unsigned spins = 0; spins < SPIN_CAP; ++spins) {
    if (ld_test(addr, want)) break;
    __builtin_amdgcn_s_sleep(1);
  }
}
__device__ __forceinline__ void ld_pub(unsigned addr, unsigned v) { asm volatile("ds_write_b32 %0, %1" :: "v"(addr), "v"(v) : "memory"); }
template <int SLP>
__device__ __forceinline__ void slow_flags(gu64* p, u64 T) {
#pragma nounroll
  for (unsigned sp = 0; sp < SPIN_CAP; ++sp) {
    if (allw(__hip_atomic_load(p, RLX_AGENT) == T)) break;
    __builtin_amdgcn_s_sleep(SLP);
  }
  asm volatile("" ::: "memory");
}
__device__ __forceinline__ void spin_flags(gu64* p, u64 T) {
#pragma nounroll
  for (unsigned sp = 0; sp < SPIN_CAP; ++sp) {
    if (allw(__hip_atomic_load(p, RLX_AGENT) == T)) break;
    __builtin_amdgcn_s_sleep(1);
  }
  asm volatile("" ::: "memory");
}
__device__ __forceinline__ void poll_flags(gu64* p, u64 first, u64 T) {
  if (!__all(first == T)) {
#pragma nounroll
    for (unsigned sp = 0; sp < SPIN_CAP; ++sp)
      if (__all(__hip_atomic_load(p, RLX_AGENT) == T)) break;
  }
  asm volatile("" ::: "memory");
}

template <bool L3>
__device__ __forceinline__ void loop_ssm(LAS char* S, int xa, const int (&oa)[5], f32x4 (&acc)[5][2], f32x4& cs0, f32x4& cs1, v4u ONES) {
  v4u xf[2][2], bf[2][5];
#define LD_S(h, B, kk)                                                                                                   \
  _Pragma("unroll") for (int f = 0; f < 2; ++f) xf[h][f] = *(volatile LAS v4u*)((B) + ((xa ^ ((kk) * 64)) + f * 2048));  \
  _Pragma("unroll") for (int i = 0; i < 5; ++i) bf[h][i] = *(volatile LAS v4u*)((B) + (oa[i] ^ ((kk) * 64)));
#define MM_S(h)                                                                     \
  _Pragma("unroll") for (int i = 0; i < 5; ++i) {                                   \
    acc[i][0] = mfma(xf[h][0], bf[h][i], acc[i][0]);                                \
    acc[i][1] = mfma(xf[h][1], bf[h][i], acc[i][1]);                                \
  }                                                                                 \
  cs0 = mfma(ONES, bf[h][0], cs0);                                                  \
  if (L3) cs1 = mfma(ONES, bf[h][1], cs1);                                          \
  _Pragma("unroll") for (int q = 0; q < 7; ++q) { SGB(0x8, 1); SGB(0x2, 1); SGB(0x100, 1); } \
  SGB(0x8, 5);
  LD_S(0, S, 0)
#pragma nounroll
  for (int s = 0; s < 10; ++s) {
    LAS char* B = S + (s & 3) * STG;
    LAS char* Bn = S + ((s + 1) & 3) * STG;
    LD_S(1, B, 1)
    MM_S(0)
    SCHED();
    BAR();
    SCHED();
    LD_S(0, Bn, 0)
    MM_S(1)
    SCHED();
  }
}

template <bool L3>
__device__ __forceinline__ void loop_ffn(LAS char* S, int xa, const int (&oa)[6], f32x4 (&acc)[6][2], f32x4& cs0, f32x4& cs1, v4u ONES) {
  v4u xf[2][2], wf[2][6];
#define LD_F(h, B, kk)                                                                                                   \
  _Pragma("unroll") for (int f = 0; f < 2; ++f) xf[h][f] = *(volatile LAS v4u*)((B) + ((xa ^ ((kk) * 64)) + f * 2048));  \
  _Pragma("unroll") for (int i = 0; i < 6; ++i) {                                                                        \
    const v2u l2 = __builtin_bit_cast(v2u, __builtin_amdgcn_ds_read_tr16_b64_v4i16((LAS v4s*)((B) + oa[i] + (kk) * 6144)));       \
    const v2u h2 = __builtin_bit_cast(v2u, __builtin_amdgcn_ds_read_tr16_b64_v4i16((LAS v4s*)((B) + oa[i] + (kk) * 6144 + 768))); \
    wf[h][i] = v4u{l2.x, l2.y, h2.x, h2.y};                                                                              \
  }
#define MM_F(h)                                                                     \
  _Pragma("unroll") for (int i = 0; i < 6; ++i) {                                   \
    acc[i][0] = mfma(wf[h][i], xf[h][0], acc[i][0]);                                \
    acc[i][1] = mfma(wf[h][i], xf[h][1], acc[i][1]);                                \
  }                                                                                 \
  if (!L3) { cs0 = mfma(ONES, wf[h][0], cs0); cs1 = mfma(ONES, wf[h][1], cs1); }    \
  _Pragma("unroll") for (int q = 0; q < 7; ++q) { SGB(0x8, 1); SGB(0x2, 1); SGB(0x100, 2); } \
  SGB(0x8, 7);
  LD_F(0, S, 0)
#pragma nounroll
  for (int s = 0; s < 10; ++s) {
    LAS char* B = S + (s & 3) * STG;
    LAS char* Bn = S + ((s + 1) & 3) * STG;
    LD_F(1, B, 1)
    MM_F(0)
    SCHED();
    BAR();
    SCHED();
    LD_F(0, Bn, 0)
    MM_F(1)
    SCHED();
  }
}

extern "C" __global__ __launch_bounds__(768) void kf(const u16* __restrict__ x, const u16* __restrict__ Lre, const u16* __restrict__ Lim,
                                                     const u16* __restrict__ Bre, const u16* __restrict__ Bim, const u16* __restrict__ Cre,
                                                     const u16* __restrict__ Cim, const u16* __restrict__ ldt, const u16* __restrict__ Dsk,
                                                     const u16* __restrict__ w1, const u16* __restrict__ b1, const u16* __restrict__ w2,
                                                     const u16* __restrict__ b2, char* __restrict__ ws, float* __restrict__ out) {
  __shared__ __attribute__((aligned(16))) char smem[K_LDS];
  LAS char* S = (LAS char*)smem;
  const int tid = threadIdx.x, lane = tid & 63, w = __builtin_amdgcn_readfirstlane(tid >> 6);
  const int c = lane & 15, g = lane >> 4;
  const int bid = blockIdx.x, xcd = bid & 7, idx = bid >> 3;
  const int rp = 4 * (xcd >> 1) + (idx & 3), cg = 8 * (xcd & 1) + (idx >> 2);
  const int rt = 4 * xcd + (idx & 3), ct = idx >> 2;
  const __amdgpu_buffer_rsrc_t rs = __builtin_amdgcn_make_buffer_rsrc((void*)ws, 0, (int)WS_END, 0x00020000);
  const int rot1 = ROTV == 0 ? (cg + 3 * rp) % 10 : ROTV == 1 ? 5 * ((cg + rp) & 1) : ROTV == 2 ? ((cg + rp) & 3) * 5 / 2 : ((cg + rp) % 3) * 3, rot2a = 0, rot2b = 0;
  STAMP_DECL STAMP(0);

  if (w < 2) {
    const int e = lane >> 3, chunk = (lane & 7) ^ e;
    const int vb = (e * D_ + 8 * chunk) * 2;
    const __amdgpu_buffer_rsrc_t rbr = __builtin_amdgcn_make_buffer_rsrc((void*)Bre, 0, P_ * D_ * 2, 0x00020000);
    const int sb = 40 * cg * D_ * 2;
    int ks = rot1;
#define P1_SYNC(st) if ((st) == 2 || (st) == 3) { WAITV(19); BAR(); } else if ((st) >= 5) { WAITV(38); BAR(); }
    if (w == 0) {
      const __amdgpu_buffer_rsrc_t rx = __builtin_amdgcn_make_buffer_rsrc((void*)x, 0, L_ * D_ * 2, 0x00020000);
      const int vx = ((8 * (e >> 2) + (e & 3)) * D_ + 8 * chunk) * 2;
      const int sx = 128 * rp * D_ * 2;
#pragma nounroll
      for (int st = 0; st < 10; ++st) {
        P1_SYNC(st)
        const int bo = (st & 3) * STG, ko = ks * 128;
#pragma unroll
        for (int p = 0; p < 16; ++p)
          DMA16(rx, bo + XOFF + p * 1024, vx, sx + ko + (32 * (p >> 2) + 16 * (p & 1) + 4 * ((p >> 1) & 1)) * D_ * 2, 0);
#pragma unroll
        for (int p = 0; p < 3; ++p) DMA16(rbr, bo + BRE + p * 1024, vb, sb + ko + 8 * p * D_ * 2, 0);
        ks = ks == 9 ? 0 : ks + 1;
      }
    } else {
      const __amdgpu_buffer_rsrc_t rbi = __builtin_amdgcn_make_buffer_rsrc((void*)Bim, 0, P_ * D_ * 2, 0x00020000);
      const __amdgpu_buffer_rsrc_t rw = __builtin_amdgcn_make_buffer_rsrc((void*)w1, 0, D_ * F_ * 2, 0x00020000);
      int vw[3];
#pragma unroll
      for (int b = 0; b < 3; ++b) {
        const int o = 1024 * b + 16 * lane, k = o / 192, bb = o % 192, seg = (bb >> 5) ^ ((k >> 3) & 1);
        vw[b] = (k * F_ + 16 * seg + 8 * ((bb >> 4) & 1)) * 2;
      }
      const int sw = 96 * cg * 2;
#pragma nounroll
      for (int st = 0; st < 10; ++st) {
        P1_SYNC(st)
        const int bo = (st & 3) * STG, ko = ks * 128;
#pragma unroll
        for (int p = 0; p < 12; ++p) DMA16(rw, bo + WOFF + p * 1024, vw[p % 3], sw + (64 * ks + 16 * (p / 3)) * F_ * 2, 0);
#pragma unroll
        for (int p = 3; p < 5; ++p) DMA16(rbr, bo + BRE + p * 1024, vb, sb + ko + 8 * p * D_ * 2, 0);
#pragma unroll
        for (int p = 0; p < 5; ++p) DMA16(rbi, bo + BIM + p * 1024, vb, sb + ko + 8 * p * D_ * 2, 0);
        ks = ks == 9 ? 0 : ks + 1;
      }
    }
    const __amdgpu_buffer_rsrc_t rcr = __builtin_amdgcn_make_buffer_rsrc((void*)Cre, 0, D_ * P_ * 2, 0x00020000);
    const __amdgpu_buffer_rsrc_t rci = __builtin_amdgcn_make_buffer_rsrc((void*)Cim, 0, D_ * P_ * 2, 0x00020000);
    int dst = 8 * 10240 + w * 5120;
#define NEXT_U() { dst += 10240; if (dst >= 14 * 10240) dst -= 14 * 10240; }
#define ISSUE_W(j) { _Pragma("unroll") for (int p = 0; p < 5; ++p) DMA16(rs, dst + p * 1024, lane * 16, WS_W2T + (ct * 48 + 2 * (j) + w) * 5120 + p * 1024, 16); NEXT_U() }
#define ISSUE_C(rc, m) { _Pragma("unroll") for (int p = 0; p < 5; ++p) DMA16(rc, dst + p * 1024, vb, (80 * ct + 40 * w + 8 * p) * P_ * 2 + (m) * 128, 0); NEXT_U() }
    WAITV(38); BAR(); STAMP(12);
    WAITV(19); BAR(); STAMP(13);
    WAITV(0); BAR(); STAMP(14);
#pragma nounroll
    for (int j = 0; j < 9; ++j) ISSUE_W(j)
    BAR(); STAMP(15);
#pragma nounroll
    for (int j = 9; j < DB; ++j) ISSUE_W(j)
    BAR();
    STAMP(1);
#pragma nounroll
    for (int j = DB; j < 24; ++j) { WAITV(55); BAR(); ISSUE_W(j) }
#pragma nounroll
    for (int m = 0; m < 10; ++m) { WAITV(55); BAR(); ISSUE_C(rcr, m) WAITV(55); BAR(); ISSUE_C(rci, m) }
    STAMP(2);
    WAITV(55); BAR(); WAITV(50); BAR(); WAITV(45); BAR(); WAITV(40); BAR(); WAITV(35); BAR(); WAITV(30); BAR();
    WAITV(25); BAR(); WAITV(20); BAR(); WAITV(15); BAR(); WAITV(10); BAR(); WAITV(5); BAR(); WAITV(0); BAR();
    STAMP(3);
    STAMP_OUT();
    return;
  }

  if (w < 4) {
    __builtin_amdgcn_s_sleep(8);
    {
      const __amdgpu_buffer_rsrc_t rw2 = __builtin_amdgcn_make_buffer_rsrc((void*)w2, 0, F_ * D_ * 2, 0x00020000);
      u16 pe[4][8];
      const int bw0 = rt < 30 ? 8 * rt + 4 * (w - 2) : 0;
#pragma unroll
      for (int i = 0; i < 4; ++i) {
        const int b = bw0 + i, so = (32 * (b / 5) * D_ + 80 * ct + 16 * (b % 5)) * 2;
#pragma unroll
        for (int j = 0; j < 8; ++j)
          pe[i][j] = __builtin_amdgcn_raw_buffer_load_b16(rw2, (4 * g * D_ + c) * 2 + (j & 3) * D_ * 2, so + 16 * (j >> 2) * D_ * 2, 0);
      }
      const unsigned ow = rt < 30 ? WS_W2T + (ct * 240 + bw0) * 1024 + lane * 16 : 0x40000000u;
#pragma unroll
      for (int i = 0; i < 4; ++i) {
        SCHED(); BAR(); SCHED();
        const v4u o = {pe[i][0] | ((unsigned)pe[i][1] << 16), pe[i][2] | ((unsigned)pe[i][3] << 16), pe[i][4] | ((unsigned)pe[i][5] << 16), pe[i][6] | ((unsigned)pe[i][7] << 16)};
        __builtin_amdgcn_raw_buffer_store_b128(o, rs, ow + i * 1024, 0, 16);
      }
    }
    STAMP(1);
    BAR();
    WAITV(0);
    BAR();
    if (w == 2) {
      const u64 T = *(LAS u64*)(S + SY_T);
      if (lane == 0) __hip_atomic_store((gu64*)(ws + WS_PT) + ct * 32 + rt, T, RLX_AGENT);
      BAR(); BAR();
      gu64* pp = (gu64*)(ws + WS_PT) + ct * 32 + (lane & 31);
      const u64 pv = __hip_atomic_load(pp, RLX_AGENT);
      BAR();
      poll_flags(pp, pv, T);
      STAMP(2);
      {
#pragma unroll
        for (int h = 0; h < 2; ++h) {
          v4u t[15];
#pragma unroll
          for (int i = 0; i < 15; ++i) t[i] = __builtin_amdgcn_raw_buffer_load_b128(rs, lane * 16, WS_W2T + (ct * 240 + 120 + (rt & 3) * 30 + h * 15 + i) * 1024, 16);
          SCHED();
          BAR();
          SCHED();
#pragma unroll
          for (int i = 0; i < 15; ++i) asm volatile("" :: "v"(t[i]));
        }
      }
      BAR();
      __builtin_amdgcn_s_sleep(FH_DELAY);
      slow_flags<FH_SLP>((gu64*)(ws + WS_FH) + rt * 32 + (lane & 31), T);
      lds_set(S, SY_HOK, 1u, lane);
      STAMP(3);
    } else {
      const u64 T = *(LAS u64*)(S + SY_T);
      bool done = false;
      const __amdgpu_buffer_rsrc_t rc = __builtin_amdgcn_make_buffer_rsrc((void*)((rt & 2) ? Cim : Cre), 0, D_ * P_ * 2, 0x00020000);
#pragma nounroll
      for (int ksp = 5; ksp < 10 + (int)SPIN_CAP; ++ksp) {
        v4u g0 = {0, 0, 0, 0}, g1 = {0, 0, 0, 0};
        if (!done) {
          g0 = __builtin_amdgcn_raw_buffer_load_b128(rs, WS_STAT + (128 * rp + lane) * 16, 0, 16);
          g1 = __builtin_amdgcn_raw_buffer_load_b128(rs, WS_STAT + (128 * rp + 64 + lane) * 16, 0, 16);
        }
        v4u u[13];
        if (ksp < 9) {
#pragma unroll
          for (int i = 0; i < 13; ++i) {
            const int blk = (ksp - 5) * 13 + i;
            u[i] = __builtin_amdgcn_raw_buffer_load_b128(rc, lane * 16, 80 * ct * P_ * 2 + ((rt & 1) * 50 + (blk < 50 ? blk : 49)) * 1024, 0);
          }
          SCHED();
          BAR();
          SCHED();
#pragma unroll
          for (int i = 0; i < 13; ++i) asm volatile("" :: "v"(u[i]));
        }
        if (!done && __all(g0.z == (unsigned)T && g0.w == (unsigned)(T >> 32) && g1.z == (unsigned)T && g1.w == (unsigned)(T >> 32))) {
          const int lr = (lane & 32) + 16 * ((lane >> 2) & 1) + 4 * ((lane >> 3) & 3) + (lane & 3);
          *(LAS unsigned*)(S + M_MRS + lr * 4) = g0.x; *(LAS unsigned*)(S + M_RS + lr * 4) = g0.y;
          *(LAS unsigned*)(S + M_MRS + (64 + lr) * 4) = g1.x; *(LAS unsigned*)(S + M_RS + (64 + lr) * 4) = g1.y;
          done = true;
        }
        if (done && ksp >= 8) break;
      }
      STAMP(2);
      BAR();
      BAR();
#pragma nounroll
      for (int i = 0; i < FS_B0; ++i) BAR();
      gu64* fp = (gu64*)(ws + WS_FS) + rt * 32 + (lane & 31);
      const u64 fv = __hip_atomic_load(fp, RLX_AGENT);
#pragma nounroll
      for (int i = FS_B0; i < 24 - DA; ++i) BAR();
      poll_flags(fp, fv, T);
      BAR();
      STAMP(3);
    }
    STAMP_OUT();
    return;
  }

  const int role = (w - 4) >> 2, mw = w & 3;
  LAS float* CS = (LAS float*)(S + M_CS);
  cpx wt[3] = {{0.f, 0.f}, {0.f, 0.f}, {0.f, 0.f}};
  if (role == 0) {
    const int q = 10 * mw + (lane < 10 ? lane : 9), ch = 40 * cg + q;
    const float lr = bf2f(Lre[ch]), li = bf2f(Lim[ch]);
    const float dt = rbf(expf(bf2f(ldt[ch])));
    const float e = expf(dt * lr);
    float sn, cn; sincosf(dt * li, &sn, &cn);
    const cpx a1 = {e * cn, e * sn};
    const float nr = a1.r - 1.f, den = 1.f / (lr * lr + li * li);
    const float cr = (nr * lr + a1.i * li) * den, ci = (a1.i * lr - nr * li) * den;
    const cpx a8 = csq(csq(csq(a1))), a16 = csq(a8), a32 = csq(a16), a24 = cmul(a8, a16);
    if (lane < 10) {
#pragma unroll
      for (int comp = 0; comp < 2; ++comp) {
        const float sg = comp ? 1.f : -1.f;
        LAS f32x4* t = (LAS f32x4*)(S + M_CT + (2 * q + comp) * 48);
        t[0] = f32x4{a1.r, sg * a1.i, comp ? -cr : cr, -ci};
        t[1] = f32x4{a8.r, sg * a8.i, a16.r, sg * a16.i};
        t[2] = f32x4{a24.r, sg * a24.i, a32.r, sg * a32.i};
      }
    }
    const cpx a128 = csq(csq(a32));
    const int ex = c < rp ? rp - 1 - c : 0;
#pragma unroll
    for (int r = 0; r < 3; ++r) {
      cpx A = {bperm((4 * r + g) * 4, a128.r), bperm((4 * r + g) * 4, a128.i)};
      cpx m = (ex & 1) ? A : cpx{1.f, 0.f};
      A = csq(A); { const cpx t = cmul(m, A); m = (ex & 2) ? t : m; }
      A = csq(A); { const cpx t = cmul(m, A); m = (ex & 4) ? t : m; }
      A = csq(A); { const cpx t = cmul(m, A); m = (ex & 8) ? t : m; }
      wt[r] = c < rp ? m : cpx{0.f, 0.f};
      asm volatile("" : "+v"(wt[r].r), "+v"(wt[r].i));
    }
  } else {
    const u64 T = __hip_atomic_load((gu64*)(ws + WS_E), RLX_AGENT) + 1ull;
    const __amdgpu_buffer_rsrc_t rx = __builtin_amdgcn_make_buffer_rsrc((void*)x, 0, L_ * D_ * 2, 0x00020000);
    const int row = 128 * rp + 8 * cg + 2 * mw + (lane >> 5), l5 = lane & 31;
    v4u xr[3];
    xr[0] = __builtin_amdgcn_raw_buffer_load_b128(rx, row * D_ * 2 + l5 * 16, 0, 0);
    xr[1] = __builtin_amdgcn_raw_buffer_load_b128(rx, row * D_ * 2 + l5 * 16, 512, 0);
    xr[2] = __builtin_amdgcn_raw_buffer_load_b128(rx, row * D_ * 2 + (l5 & 15) * 16, 1024, 0);
    if (w == 8 && lane < 48) *(LAS unsigned*)(S + M_B1 + 4 * lane) = *(const GAS unsigned*)(b1 + 96 * cg + 2 * lane);
    if (w == 9 && lane == 0) {
      *(LAS u64*)(S + SY_T) = T;
      *(LAS unsigned*)(S + SY_C3) = 0u;
      *(LAS v4u*)(S + SY_C1) = v4u{0, 0, 0x3f800000u, 0};
#pragma unroll
      for (int i = 1; i < 5; ++i) *(LAS v4u*)(S + M_SYNC + 16 * i) = v4u{0, 0, 0, 0};
      __hip_atomic_store((gu64*)(ws + WS_RDY) + bid, T, RLX_AGENT);
    }
    const float wl = l5 < 16 ? 1.f : 0.f;
    float s0 = 0.f, s2 = 0.f, u_, v_;
#pragma unroll
    for (int e = 0; e < 4; ++e) {
      s0 += bflo(xr[0][e]) + bfhi(xr[0][e]); s0 += bflo(xr[1][e]) + bfhi(xr[1][e]); s2 += bflo(xr[2][e]) + bfhi(xr[2][e]);
    }
    float sm_ = fmaf(s2, wl, s0);
    sm_ += dpp<0x128>(sm_); sm_ += dpp<0x124>(sm_); sm_ += dpp<0x122>(sm_); sm_ += dpp<0x121>(sm_);
    rows16(sm_, u_, v_); sm_ = u_ + v_;
    const float m = sm_ * (1.f / D_);
    float q0 = 0.f, q2 = 0.f;
#pragma unroll
    for (int e = 0; e < 4; ++e) {
      float d;
      d = bflo(xr[0][e]) - m; q0 = fmaf(d, d, q0); d = bfhi(xr[0][e]) - m; q0 = fmaf(d, d, q0);
      d = bflo(xr[1][e]) - m; q0 = fmaf(d, d, q0); d = bfhi(xr[1][e]) - m; q0 = fmaf(d, d, q0);
      d = bflo(xr[2][e]) - m; q2 = fmaf(d, d, q2); d = bfhi(xr[2][e]) - m; q2 = fmaf(d, d, q2);
    }
    float q = fmaf(q2, wl, q0);
    q += dpp<0x128>(q); q += dpp<0x124>(q); q += dpp<0x122>(q); q += dpp<0x121>(q);
    rows16(q, u_, v_); q = u_ + v_;
    const float mb = rbf(m), sd = rbf(__builtin_amdgcn_sqrtf(rbf(rbf(q * (1.f / D_)) + 1e-5f)));
    const float ri = __builtin_amdgcn_rcpf(sd);
    if (l5 == 0)
      __builtin_amdgcn_raw_buffer_store_b128(v4u{asu(-mb * ri), asu(ri), (unsigned)T, (unsigned)(T >> 32)}, rs, WS_STAT + row * 16, 0, 16);
  }
  const int xa = XOFF + (32 * mw + c) * 128 + ((g ^ (c & 7)) << 4);
  f32x4 cs0 = {0, 0, 0, 0}, cs1 = {0, 0, 0, 0};
  v4u ONES = {0x3f803f80u, 0x3f803f80u, 0x3f803f80u, 0x3f803f80u};
  asm volatile("" : "+v"(ONES));
  STAMP(1);
  BAR();
  STAMP(2);
  const unsigned fslot = (2 * rp + (mw >> 1)) * 32 + cg * 2 + (mw & 1);
  const int tok = 64 * rt + 32 * (mw >> 1) + 8 * (c >> 2) + 4 * (mw & 1) + (c & 3);
  LAS char* X = S + 2 * SL2 + mw * 5120 + lane * 16;

  if (role == 0) {
    int oa[5], col[5];
#pragma unroll
    for (int i = 0; i < 5; ++i) {
      int j = i + mw; j = j >= 5 ? j - 5 : j;
      col[i] = 16 * j + c;
      oa[i] = ((c & 1) ? BIM : BRE) + (8 * j + (c >> 1)) * 128 + ((g ^ (c >> 1)) << 4);
    }
    f32x4 acc[5][2];
#pragma unroll
    for (int i = 0; i < 5; ++i) { acc[i][0] = f32x4{0, 0, 0, 0}; acc[i][1] = f32x4{0, 0, 0, 0}; }
    if (mw == 3) loop_ssm<true>(S, xa, oa, acc, cs0, cs1, ONES);
    else loop_ssm<false>(S, xa, oa, acc, cs0, cs1, ONES);
    STAMP(3);
    __builtin_amdgcn_s_setprio(2);
    if (g == 0) {
      CS[16 * mw + c] = cs0[0];
      if (mw == 3) CS[64 + c] = cs1[0];
    }
    f32x4 k0[5], k1_[5];
#pragma unroll
    for (int i = 0; i < 5; ++i) {
      k0[i] = *(LAS f32x4*)(S + M_CT + col[i] * 48);
      k1_[i] = *(LAS f32x4*)(S + M_CT + col[i] * 48 + 16);
    }
    const u64 T = *(LAS u64*)(S + SY_T);
    BAR();
    STAMP(4);
    float v[5][8], E[5];
    {
      f32x4 mt4[2], rt4[2];
      float cs[5];
#pragma unroll
      for (int f = 0; f < 2; ++f) {
        mt4[f] = *(LAS f32x4*)(S + M_MRS + (32 * mw + 16 * f + 4 * g) * 4);
        rt4[f] = *(LAS f32x4*)(S + M_RS + (32 * mw + 16 * f + 4 * g) * 4);
      }
#pragma unroll
      for (int i = 0; i < 5; ++i) cs[i] = CS[col[i]];
#pragma unroll
      for (int f = 0; f < 2; ++f)
#pragma unroll
        for (int i = 0; i < 5; ++i) {
          const f32x4 t = acc[i][f] * rt4[f] + mt4[f] * cs[i];
#pragma unroll
          for (int r = 0; r < 4; ++r) v[i][4 * f + r] = t[r];
        }
      float ar[5], ai[5];
#pragma unroll
      for (int i = 0; i < 5; ++i) { ar[i] = k0[i].x; ai[i] = k0[i].y; }
#pragma unroll
      for (int s = 1; s < 8; ++s)
        asm volatile("v_fmac_f32 %0, %10, %5\n\tv_fmac_f32 %1, %11, %6\n\tv_fmac_f32 %2, %12, %7\n\tv_fmac_f32 %3, %13, %8\n\tv_fmac_f32 %4, %14, %9\n\t"
                     "v_fmac_f32_dpp %0, %5, %15 quad_perm:[1,0,3,2] row_mask:0xf bank_mask:0xf\n\t"
                     "v_fmac_f32_dpp %1, %6, %16 quad_perm:[1,0,3,2] row_mask:0xf bank_mask:0xf\n\t"
                     "v_fmac_f32_dpp %2, %7, %17 quad_perm:[1,0,3,2] row_mask:0xf bank_mask:0xf\n\t"
                     "v_fmac_f32_dpp %3, %8, %18 quad_perm:[1,0,3,2] row_mask:0xf bank_mask:0xf\n\t"
                     "v_fmac_f32_dpp %4, %9, %19 quad_perm:[1,0,3,2] row_mask:0xf bank_mask:0xf\n\ts_nop 1"
                     : "+v"(v[0][s]), "+v"(v[1][s]), "+v"(v[2][s]), "+v"(v[3][s]), "+v"(v[4][s])
                     : "v"(v[0][s - 1]), "v"(v[1][s - 1]), "v"(v[2][s - 1]), "v"(v[3][s - 1]), "v"(v[4][s - 1]),
                       "v"(ar[0]), "v"(ar[1]), "v"(ar[2]), "v"(ar[3]), "v"(ar[4]), "v"(ai[0]), "v"(ai[1]), "v"(ai[2]), "v"(ai[3]), "v"(ai[4]));
      float r0[5], q0[5], W[5];
#pragma unroll
      for (int i = 0; i < 5; ++i) {
        float r1, q1;
        rows16(v[i][7], r0[i], r1);
        const float Pp = r1 + cm(k1_[i].x, k1_[i].y, r0[i]);
        rows32(Pp, q0[i], q1);
        W[i] = q1 + cm(k1_[i].z, k1_[i].w, q0[i]);
      }
      if (g == 0) {
#pragma unroll
        for (int i = 0; i < 5; ++i) *(LAS float*)(S + M_WAGG + (mw * 80 + col[i]) * 4) = W[i];
      }
      WAITL();
      unsigned old = 0;
      if (lane == 0) old = __hip_atomic_fetch_add((LAS unsigned*)(S + SY_C1), 1u, RLX_WG);
#pragma unroll
      for (int i = 0; i < 5; ++i) {
        const float X = r0[i] + cm(k1_[i].x, k1_[i].y, q0[i]);
        const float lo = (g & 1) ? r0[i] : 0.f, hi = (g & 1) ? X : q0[i];
        E[i] = (g & 2) ? hi : lo;
      }
      old = __builtin_amdgcn_readfirstlane(old);
      if (old == 3) {
        asm volatile("" ::: "memory");
        const int q = lane < 40 ? lane : 39;
        const f32x2 k2 = *(LAS f32x2*)(S + M_CT + (2 * q + 1) * 48 + 40);
        f32x2 wv[4];
#pragma unroll
        for (int ww = 0; ww < 4; ++ww) wv[ww] = *(LAS f32x2*)(S + M_WAGG + (ww * 80 + 2 * q) * 4);
        const cpx a32 = {k2.x, k2.y};
        cpx ag = {wv[0].x, wv[0].y};
#pragma unroll
        for (int ww = 1; ww < 4; ++ww) { ag = cmul(ag, a32); ag.r += wv[ww].x; ag.i += wv[ww].y; }
        if (lane < 40)
          __builtin_amdgcn_raw_buffer_store_b128(v4u{asu(ag.r), asu(ag.i), (unsigned)T, (unsigned)(T >> 32)}, rs,
                                                 WS_AGG + ((cg * 40 + q) * 16 + rp) * 16, 0, 16);
      }
    }
    STAMP(5);
#pragma unroll
    for (int s = 0; s < 8; ++s)
#pragma unroll
      for (int i = 0; i < 5; ++i) v[i][s] = cm(k0[i].z, k0[i].w, v[i][s]);
#pragma unroll
    for (int i = 0; i < 5; ++i)
      asm volatile("" : "+v"(v[i][0]), "+v"(v[i][1]), "+v"(v[i][2]), "+v"(v[i][3]), "+v"(v[i][4]), "+v"(v[i][5]), "+v"(v[i][6]), "+v"(v[i][7]));
    f32x2 a32[5], ag8[5];
#pragma unroll
    for (int i = 0; i < 5; ++i) {
      a32[i] = *(LAS f32x2*)(S + M_CT + col[i] * 48 + 40);
      ag8[i] = *(LAS f32x2*)(S + (g ? M_CT + col[i] * 48 + 8 + 8 * g : SY_ONE));
    }
    {
      int chn[3];
      const bool act = c < rp;
#pragma unroll
      for (int r = 0; r < 3; ++r) chn[r] = 10 * mw + (4 * r + g < 10 ? 4 * r + g : 9);
      STAMP(6);
      v4u gr[3] = {{0, 0, 0, 0}, {0, 0, 0, 0}, {0, 0, 0, 0}};
      if (rp > 0) {
        gu64* tp = (gu64*)(ws + WS_AGG + ((cg * 40 + 10 * mw) * 16 + (act ? c : 0)) * 16 + 8);
#pragma nounroll
        for (unsigned sp = 0; sp < SPIN_CAP; ++sp) {
          if (__all(__hip_atomic_load(tp, RLX_AGENT) == T)) break;
          __builtin_amdgcn_s_sleep(1);
        }
#pragma nounroll
        for (unsigned sp = 0; sp < SPIN_CAP; ++sp) {
          asm volatile("" ::: "memory");
          bool ok = true;
#pragma unroll
          for (int r = 0; r < 3; ++r) gr[r] = __builtin_amdgcn_raw_buffer_load_b128(rs, WS_AGG + ((cg * 40 + chn[r]) * 16 + c) * 16, 0, 16);
#pragma unroll
          for (int r = 0; r < 3; ++r) ok = ok && gr[r].z == (unsigned)T && gr[r].w == (unsigned)(T >> 32);
          if (__all(ok || !act)) break;
        }
      }
      STAMP(7);
      cpx h[3];
#pragma unroll
      for (int r = 0; r < 3; ++r) {
        const cpx ag = {act ? asf(gr[r].x) : 0.f, act ? asf(gr[r].y) : 0.f};
        h[r] = cmul(wt[r], ag);
      }
#pragma unroll
      for (int r = 0; r < 3; ++r) { h[r].r += dpp<0x128>(h[r].r); h[r].i += dpp<0x128>(h[r].i); }
#pragma unroll
      for (int r = 0; r < 3; ++r) { h[r].r += dpp<0x124>(h[r].r); h[r].i += dpp<0x124>(h[r].i); }
#pragma unroll
      for (int r = 0; r < 3; ++r) { h[r].r += dpp<0x122>(h[r].r); h[r].i += dpp<0x122>(h[r].i); }
#pragma unroll
      for (int r = 0; r < 3; ++r) { h[r].r += dpp<0x121>(h[r].r); h[r].i += dpp<0x121>(h[r].i); }
      if (c == 0) {
#pragma unroll
        for (int r = 0; r < 3; ++r) *(LAS f32x2*)(S + M_HIN + chn[r] * 8) = f32x2{h[r].r, h[r].i};
      }
      WAITL();
      if (lane == 0) __hip_atomic_fetch_add((LAS unsigned*)(S + SY_C2), 1u, RLX_WG);
      lds_wait_ge(S, SY_C2, 4u);
    }
    STAMP(8);
    {
      float cw[5];
#pragma unroll
      for (int i = 0; i < 5; ++i) cw[i] = *(LAS float*)(S + M_HIN + col[i] * 4);
      float wa[3][5];
#pragma unroll
      for (int ww = 0; ww < 3; ++ww)
#pragma unroll
        for (int i = 0; i < 5; ++i) wa[ww][i] = *(LAS float*)(S + M_WAGG + (ww * 80 + col[i]) * 4);
#pragma unroll
      for (int ww = 0; ww < 3; ++ww) {
        if (ww < mw) {
#pragma unroll
          for (int i = 0; i < 5; ++i) cw[i] = cm(a32[i].x, a32[i].y, cw[i]) + wa[ww][i];
        }
      }
#pragma unroll
      for (int i = 0; i < 5; ++i) cw[i] = cm(ag8[i].x, ag8[i].y, cw[i]) + E[i];
#pragma unroll
      for (int i = 0; i < 5; ++i) cw[i] = cm(k0[i].z, k0[i].w, cw[i]);
#pragma unroll
      for (int s = 0; s < 8; ++s)
#pragma unroll
        for (int i = 0; i < 5; ++i) { cw[i] = cm(k0[i].x, -k0[i].y, cw[i]); v[i][s] += cw[i]; }
      LAS char* IMG = S + M_IMG + mw * 3072;
      LAS char* wa_ = IMG + c * 64 + ((g ^ ((c >> 1) & 3)) << 4);
      const int q = c >> 2, p = c & 3, f = g >> 1, comp = g & 1;
      LAS char* ra_ = IMG + (2 * q + comp) * 64 + ((p ^ q) << 4) + 8 * f;
      const unsigned ob = WS_HS + ((2 * rp + (mw >> 1)) * 160 + comp * 8 + 2 * (mw & 1) + f) * 1024 + c * 16;
#pragma unroll
      for (int h = 0; h < 2; ++h) {
#pragma unroll
        for (int i = 3 * h; i < (h ? 5 : 3); ++i)
          *(LAS v4u*)(wa_ + (i - 3 * h) * 1024) = v4u{pk2(v[i][0], v[i][1]), pk2(v[i][2], v[i][3]), pk2(v[i][4], v[i][5]), pk2(v[i][6], v[i][7])};
#pragma unroll
        for (int i = 3 * h; i < (h ? 5 : 3); ++i) {
          const v2u lo = __builtin_bit_cast(v2u, __builtin_amdgcn_ds_read_tr16_b64_v4i16((LAS v4s*)(ra_ + (i - 3 * h) * 1024)));
          const v2u hi = __builtin_bit_cast(v2u, __builtin_amdgcn_ds_read_tr16_b64_v4i16((LAS v4s*)(ra_ + (i - 3 * h) * 1024 + 512)));
          int j = i + mw; j = j >= 5 ? j - 5 : j;
          const int GO = 5 * cg + j;
          __builtin_amdgcn_raw_buffer_store_b128(v4u{lo.x, lo.y, hi.x, hi.y}, rs, ob, ((GO >> 3) * 16 + ((GO >> 2) & 1) * 4) * 1024 + (GO & 3) * 256, 16);
        }
      }
    }
    __builtin_amdgcn_s_setprio(0);
    STAMP(9);
  } else {
    const int rot = mw < 2 ? mw : 2;
    int oa[6];
#pragma unroll
    for (int i = 0; i < 6; ++i) {
      int pp = (i >> 1) + rot; pp = pp >= 3 ? pp - 3 : pp;
      oa[i] = WOFF + (8 * g + (c >> 2)) * 192 + 64 * pp + 32 * ((i & 1) ^ (g & 1)) + 8 * (c & 3);
    }
    f32x4 acc[6][2];
#pragma unroll
    for (int i = 0; i < 6; ++i) { acc[i][0] = f32x4{0, 0, 0, 0}; acc[i][1] = f32x4{0, 0, 0, 0}; }
    if (mw == 3) loop_ffn<true>(S, xa, oa, acc, cs0, cs1, ONES);
    else loop_ffn<false>(S, xa, oa, acc, cs0, cs1, ONES);
    STAMP(3);
    if (g == 0 && mw != 3) {
      CS[80 + 32 * rot + c] = cs0[0];
      CS[80 + 32 * rot + 16 + c] = cs1[0];
    }
    BAR();
    STAMP(4);
    float mrs[2], rsd[2];
#pragma unroll
    for (int f = 0; f < 2; ++f) {
      mrs[f] = *(LAS float*)(S + M_MRS + (32 * mw + 16 * f + c) * 4);
      rsd[f] = *(LAS float*)(S + M_RS + (32 * mw + 16 * f + c) * 4);
    }
#pragma unroll
    for (int pp = 0; pp < 3; ++pp) {
      int pq = pp + rot; pq = pq >= 3 ? pq - 3 : pq;
      unsigned o[2][4];
#pragma unroll
      for (int e = 0; e < 2; ++e) {
        const f32x4 cs = *(LAS f32x4*)(S + M_CS + (80 + 32 * pq + 16 * e + 4 * g) * 4);
        const v2u bb = *(LAS v2u*)(S + M_B1 + (32 * pq + 16 * e + 4 * g) * 2);
        const f32x4 bv = {bflo(bb.x), bfhi(bb.x), bflo(bb.y), bfhi(bb.y)};
#pragma unroll
        for (int f = 0; f < 2; ++f) {
          const f32x4 t = acc[2 * pp + e][f] * rsd[f] + (cs * mrs[f] + bv);
          const f32x4 z = t * (t * t * (0.044715f * -2.302208198f) + -2.302208198f);
          f32x4 d;
#pragma unroll
          for (int r = 0; r < 4; ++r) d[r] = __builtin_amdgcn_exp2f(z[r]);
          d = d + 1.f;
#pragma unroll
          for (int r = 0; r < 4; ++r) d[r] = __builtin_amdgcn_rcpf(d[r]);
          const f32x4 hv = t * d;
          o[f][2 * e] = pk2(hv[0], hv[1]); o[f][2 * e + 1] = pk2(hv[2], hv[3]);
        }
      }
#pragma unroll
      for (int f = 0; f < 2; ++f)
        __builtin_amdgcn_raw_buffer_store_b128(v4u{o[f][0], o[f][1], o[f][2], o[f][3]}, rs,
                                               WS_HF + (((2 * rp + (mw >> 1)) * 48 + 3 * cg + pq) * 4 + 2 * (mw & 1) + f) * 1024 + lane * 16, 0, 16);
    }
    STAMP(9);
    WAITV(0);
    const u64 T = *(LAS u64*)(S + SY_T);
    if (lane == 0) __hip_atomic_store((gu64*)(ws + WS_FH) + fslot, T, RLX_AGENT);
    STAMP(10);
    if (bid == 0 && w == 8) {
      gu64* rd = (gu64*)(ws + WS_RDY) + lane;
#pragma nounroll
      for (unsigned sp = 0; sp < SPIN_CAP; ++sp) {
        const u64 a = __hip_atomic_load(rd, RLX_AGENT), b = __hip_atomic_load(rd + 64, RLX_AGENT),
                  cc = __hip_atomic_load(rd + 128, RLX_AGENT), d = __hip_atomic_load(rd + 192, RLX_AGENT);
        if (__all(a == T && b == T && cc == T && d == T)) break;
        __builtin_amdgcn_s_sleep(4);
      }
      if (lane == 0) __hip_atomic_store((gu64*)(ws + WS_E), T, RLX_AGENT);
    }
  }
  lds_wait_sl(S, SY_HOK, 1u);
  STAMP(11);

  f32x4 accF[5], accS[5];
#pragma unroll
  for (int n = 0; n < 5; ++n) { accF[n] = f32x4{0, 0, 0, 0}; accS[n] = f32x4{0, 0, 0, 0}; }
  v2u xv[3], dv[3], bv[3];
  v4u sg;
  {
    const u64 Tq = *(LAS u64*)(S + SY_T);
    const int av = (role * 4 + mw) * 1024 + lane * 16;
    const int aF = WS_HF + rt * 48 * 4096, aS = WS_HS + rt * 40 * 4096 - 24 * 8192;
    const int blF = role * 5120 + lane * 16, blS = c * 128 + (((4 * role + g) ^ (c & 7)) << 4);
    v4u a[DA], b[3][5];
#define LDA2(j) a[(j) % DA] = __builtin_amdgcn_raw_buffer_load_b128(rs, av, ((j) < 24 ? aF : aS) + (j) * 8192, 16)
#pragma unroll
    for (int j = 0; j < DA; ++j) LDA2(j);
#pragma unroll
    for (int j = 0; j <= NU + 1; ++j) {
      if (j < NU) {
        asm volatile("s_waitcnt lgkmcnt(5)" ::: "memory");
        __builtin_amdgcn_s_barrier();
        asm volatile("" ::: "memory");
        if (j == 0) STAMP(12);
        int bp = (j < 24 ? blF : blS) + slotu(j);
        asm volatile("" : "+v"(bp));
#pragma unroll
        for (int n = 0; n < 5; ++n) b[j % 3][n] = *(volatile LAS v4u*)(S + bp + n * (j < 24 ? 1024 : 2048));
      }
      SCHED();
      if (j > 1) {
#pragma unroll
        for (int n = 0; n < 5; ++n) {
          if (j - 2 < 24) accF[n] = mfma(b[(j - 2) % 3][n], a[(j - 2) % DA], accF[n]);
          else accS[n] = mfma(b[(j - 2) % 3][n], a[(j - 2) % DA], accS[n]);
        }
        if (j - 2 + DA < NU) LDA2(j - 2 + DA);
      }
      if (j == 2 && role == 0) {
        asm volatile("s_waitcnt vmcnt(%0)" :: "i"(DA) : "memory");
        if (lane == 0) __hip_atomic_store((gu64*)(ws + WS_FS) + fslot, Tq, RLX_AGENT);
      }
      if (j == NU - DA + 3) {
#pragma unroll
        for (int n = 0; n < 3; ++n) {
          const int nn = role ? (n < 2 ? 3 + n : 4) : n, d = 80 * ct + 16 * nn + 4 * g;
          xv[n] = *(const GAS v2u*)(x + tok * D_ + d);
          dv[n] = *(const GAS v2u*)(Dsk + d);
          bv[n] = *(const GAS v2u*)(b2 + d);
        }
        sg = __builtin_amdgcn_raw_buffer_load_b128(rs, WS_STAT + tok * 16, 0, 16);
      }
      SCHED();
    }
  }
  STAMP(13);
  {
    LAS char* Xc = S + lane * 16;
    if (role == 0) {
#pragma unroll
      for (int n = 3; n < 5; ++n) {
        *(LAS f32x4*)(Xc + (mw * 10 + n) * 1024) = accF[n];
        *(LAS f32x4*)(Xc + (mw * 10 + 5 + n) * 1024) = accS[n];
      }
    } else {
#pragma unroll
      for (int n = 0; n < 3; ++n) {
        *(LAS f32x4*)(Xc + (mw * 10 + n) * 1024) = accF[n];
        *(LAS f32x4*)(Xc + (mw * 10 + 5 + n) * 1024) = accS[n];
      }
      accF[0] = accF[3]; accF[1] = accF[4]; accF[2] = accF[4];
      accS[0] = accS[3]; accS[1] = accS[4]; accS[2] = accS[4];
    }
    {
      const u64 T = *(LAS u64*)(S + SY_T);
#pragma nounroll
      for (unsigned sp = 0; sp < SPIN_CAP && !allw(sg.z == (unsigned)T && sg.w == (unsigned)(T >> 32)); ++sp) {
        asm volatile("" ::: "memory");
        sg = __builtin_amdgcn_raw_buffer_load_b128(rs, WS_STAT + tok * 16, 0, 16);
      }
    }
    BAR();
    STAMP(14);
    const float mrs = asf(sg.x), rsd = asf(sg.y);
    const __amdgpu_buffer_rsrc_t ro = __builtin_amdgcn_make_buffer_rsrc((void*)out, 0, L_ * D_ * 4, 0x00020000);
#pragma unroll
    for (int n = 0; n < 3; ++n) {
      const int nn = role ? (n < 2 ? 3 + n : 4) : n;
      const f32x4 fs = accF[n] + *(LAS f32x4*)(Xc + (mw * 10 + nn) * 1024);
      const f32x4 ss = accS[n] + *(LAS f32x4*)(Xc + (mw * 10 + 5 + nn) * 1024);
      const f32x4 xs = {bflo(xv[n].x), bfhi(xv[n].x), bflo(xv[n].y), bfhi(xv[n].y)};
      const f32x4 ds = {bflo(dv[n].x), bfhi(dv[n].x), bflo(dv[n].y), bfhi(dv[n].y)};
      const f32x4 bs = {bflo(bv[n].x), bfhi(bv[n].x), bflo(bv[n].y), bfhi(bv[n].y)};
      const f32x4 vv = fs + bs;
      f32x4 d = vv * -1.442695041f;
#pragma unroll
      for (int r = 0; r < 4; ++r) d[r] = __builtin_amdgcn_exp2f(d[r]);
      d = d + 1.f;
#pragma unroll
      for (int r = 0; r < 4; ++r) d[r] = __builtin_amdgcn_rcpf(d[r]);
      const f32x4 gg = vv * d;
      const f32x4 o = ((xs * rsd + mrs) * ds + ss * 2.f) * gg + xs;
      if (role == 0 || n < 2) __builtin_amdgcn_raw_buffer_store_b128(__builtin_bit_cast(v4u, o), ro, (tok * D_ + 80 * ct + 16 * nn + 4 * g) * 4, 0, OUT_AUX);
    }
  }
  STAMP(15);
  STAMP(16);
  STAMP_OUT();
}

extern "C" void kernel_launch(void* const* d_in, const int* in_sizes, int n_in, void* d_out, int out_size, void* d_ws, size_t ws_size,
                              hipStream_t stream) {
  kf<<<256, 768, 0, stream>>>((const u16*)d_in[0], (const u16*)d_in[1], (const u16*)d_in[2], (const u16*)d_in[3], (const u16*)d_in[4],
                              (const u16*)d_in[5], (const u16*)d_in[6], (const u16*)d_in[7], (const u16*)d_in[8], (const u16*)d_in[10],
                              (const u16*)d_in[11], (const u16*)d_in[12], (const u16*)d_in[13], (char*)d_ws, (float*)d_out);
}
```

```cpp
#include <hip/hip_runtime.h>
#include <stdint.h>

typedef unsigned short u16;
typedef unsigned long long u64;
typedef unsigned v4u __attribute__((ext_vector_type(4)));
typedef unsigned v2u __attribute__((ext_vector_type(2)));
typedef float f32x4 __attribute__((ext_vector_type(4)));
typedef float f32x2 __attribute__((ext_vector_type(2)));
typedef short bf16x8 __attribute__((ext_vector_type(8)));
typedef short v4s __attribute__((ext_vector_type(4)));
typedef __bf16 v2bf __attribute__((ext_vector_type(2)));
#define GAS __attribute__((address_space(1)))
#define LAS __attribute__((address_space(3)))
typedef GAS u64 gu64;
#define RLX_AGENT __ATOMIC_RELAXED, __HIP_MEMORY_SCOPE_AGENT
#define RLX_WG __ATOMIC_RELAXED, __HIP_MEMORY_SCOPE_WORKGROUP

constexpr int L_ = 2048, D_ = 640, P_ = 640, F_ = 1536;
constexpr unsigned SPIN_CAP = 1u << 20;
constexpr unsigned WS_E = 0;
constexpr unsigned WS_PT = 4096;
constexpr unsigned WS_FH = 8192;
constexpr unsigned WS_FS = 16384;
constexpr unsigned WS_STAT = 32768;
constexpr unsigned WS_AGG = 65536;
constexpr unsigned WS_HF = WS_AGG + 16 * 40 * 16 * 16;
constexpr unsigned WS_HS = WS_HF + 32 * 48 * 4096;
constexpr unsigned WS_W2T = WS_HS + 32 * 40 * 4096;
constexpr unsigned WS_END = WS_W2T + 8 * 48 * 5120;

__device__ __forceinline__ float bf2f(unsigned v16) { return __builtin_bit_cast(float, v16 << 16); }
__device__ __forceinline__ float bflo(unsigned v) { return __builtin_bit_cast(float, v << 16); }
__device__ __forceinline__ float bfhi(unsigned v) { return __builtin_bit_cast(float, v & 0xffff0000u); }
__device__ __forceinline__ unsigned pk2(float a, float b) { v2bf r = {(__bf16)a, (__bf16)b}; return __builtin_bit_cast(unsigned, r); }
__device__ __forceinline__ float asf(unsigned v) { return __builtin_bit_cast(float, v); }
__device__ __forceinline__ unsigned asu(float v) { return __builtin_bit_cast(unsigned, v); }
__device__ __forceinline__ float rbf(float a) { return (float)(__bf16)a; }
template <int CTRL> __device__ __forceinline__ float dpp(float x) {
  return __builtin_bit_cast(float, __builtin_amdgcn_mov_dpp(__builtin_bit_cast(int, x), CTRL, 0xf, 0xf, true));
}
__device__ __forceinline__ float swp(float x) { return dpp<0xB1>(x); }
__device__ __forceinline__ float cm(float mr, float mi, float v) { return fmaf(mr, v, mi * swp(v)); }
__device__ __forceinline__ float bperm(int byteaddr, float v) {
  return __builtin_bit_cast(float, __builtin_amdgcn_ds_bpermute(byteaddr, __builtin_bit_cast(int, v)));
}
__device__ __forceinline__ void rows16(float x, float& ev, float& od) {
  const auto r = __builtin_amdgcn_permlane16_swap(asu(x), asu(x), false, false); ev = asf(r[0]); od = asf(r[1]);
}
__device__ __forceinline__ void rows32(float x, float& lo, float& hi) {
  const auto r = __builtin_amdgcn_permlane32_swap(asu(x), asu(x), false, false); lo = asf(r[0]); hi = asf(r[1]);
}
__device__ __forceinline__ f32x4 mfma(v4u a, v4u b, f32x4 c) {
  return __builtin_amdgcn_mfma_f32_16x16x32_bf16(__builtin_bit_cast(bf16x8, a), __builtin_bit_cast(bf16x8, b), c, 0, 0, 0);
}
#define WAITV(n) asm volatile("s_waitcnt vmcnt(" #n ")" ::: "memory")
#define WAITL() asm volatile("s_waitcnt lgkmcnt(0)" ::: "memory")
#define BAR() do { WAITL(); __builtin_amdgcn_s_barrier(); asm volatile("" ::: "memory"); } while (0)
#define SGB(mask, n) __builtin_amdgcn_sched_group_barrier(mask, n, 0)
#define SCHED() __builtin_amdgcn_sched_barrier(0)
struct cpx { float r, i; };
__device__ __forceinline__ cpx cmul(cpx a, cpx b) { return {a.r * b.r - a.i * b.i, a.r * b.i + a.i * b.r}; }
__device__ __forceinline__ cpx csq(cpx a) { return {a.r * a.r - a.i * a.i, 2.f * a.r * a.i}; }

constexpr int FS_B0 = 5;
constexpr int PFA = 6, PFB = 9;
constexpr int FH_DELAY = 80, FH_SLP = 8;
constexpr int OUT_AUX = 2;
constexpr int XOFF = 0, BRE = 16384, BIM = BRE + 5120 + 128, WOFF = BIM + 5120, STG = WOFF + 12288, MISC = 4 * STG;
constexpr int M_CT = MISC, M_MRS = M_CT + 80 * 48, M_RS = M_MRS + 512, M_CS = M_RS + 512, M_WAGG = M_CS + 704, M_B1 = M_WAGG + 1280,
              M_HIN = M_B1 + 192, M_SYNC = M_HIN + 320, K_LDS = M_SYNC + 96;
static_assert(K_LDS <= 163840, "lds");
constexpr int SY_C1 = M_SYNC, SY_C2 = M_SYNC + 4, SY_ONE = M_SYNC + 8, SY_HOK = M_SYNC + 24, SY_T = M_SYNC + 80;
constexpr int SL2 = 20480, M_IMG = 7 * SL2;
constexpr int DA = 10;
constexpr int NU = 44, DB = 12;
__device__ __forceinline__ constexpr int slotu(int j) { return ((j + 8) % 14) * 10240; }
static_assert(M_IMG + 4 * 3072 <= MISC, "img");

#define DMA16(rsrc, ldsoff, voff, soff, aux) __builtin_amdgcn_raw_ptr_buffer_load_lds(rsrc, (LAS void*)(S + (ldsoff)), 16, voff, soff, 0, aux)

__device__ __forceinline__ bool allw(bool p) { return __builtin_amdgcn_ballot_w64(p) == ~0ull; }
__device__ __forceinline__ void lds_wait_ge(LAS char* S, int off, unsigned want) {
#pragma nounroll
  for (unsigned spins = 0; spins < SPIN_CAP; ++spins) {
    if (allw(__hip_atomic_load((LAS unsigned*)(S + off), RLX_WG) >= want)) break;
    __builtin_amdgcn_s_sleep(1);
  }
  asm volatile("" ::: "memory");
}
__device__ __forceinline__ void lds_wait_sl(LAS char* S, int off, unsigned want) {
#pragma nounroll
  for (unsigned spins = 0; spins < SPIN_CAP; ++spins) {
    if (allw(__hip_atomic_load((LAS unsigned*)(S + off), RLX_WG) >= want)) break;
    __builtin_amdgcn_s_sleep(4);
  }
  asm volatile("" ::: "memory");
}
__device__ __forceinline__ void lds_set(LAS char* S, int off, unsigned v, int lane) {
  if (lane == 0) __hip_atomic_store((LAS unsigned*)(S + off), v, RLX_WG);
}
template <int SLP>
__device__ __forceinline__ void slow_flags(gu64* p, u64 T) {
#pragma nounroll
  for (unsigned sp = 0; sp < SPIN_CAP; ++sp) {
    if (allw(__hip_atomic_load(p, RLX_AGENT) == T)) break;
    __builtin_amdgcn_s_sleep(SLP);
  }
  asm volatile("" ::: "memory");
}
__device__ __forceinline__ void poll_flags(gu64* p, u64 first, u64 T) {
  if (!__all(first == T)) {
#pragma nounroll
    for (unsigned sp = 0; sp < SPIN_CAP; ++sp)
      if (__all(__hip_atomic_load(p, RLX_AGENT) == T)) break;
  }
  asm volatile("" ::: "memory");
}

template <bool L3>
__device__ __forceinline__ void loop_ssm(LAS char* S, int xa, const int (&oa)[5], f32x4 (&acc)[5][2], f32x4& cs0, f32x4& cs1, v4u ONES) {
  v4u xf[2][2], bf[2][5];
#define LD_S(h, B, kk)                                                                                                   \
  _Pragma("unroll") for (int f = 0; f < 2; ++f) xf[h][f] = *(volatile LAS v4u*)((B) + ((xa ^ ((kk) * 64)) + f * 2048));  \
  _Pragma("unroll") for (int i = 0; i < 5; ++i) bf[h][i] = *(volatile LAS v4u*)((B) + (oa[i] ^ ((kk) * 64)));
#define MM_S(h)                                                                     \
  _Pragma("unroll") for (int i = 0; i < 5; ++i) {                                   \
    acc[i][0] = mfma(xf[h][0], bf[h][i], acc[i][0]);                                \
    acc[i][1] = mfma(xf[h][1], bf[h][i], acc[i][1]);                                \
  }                                                                                 \
  cs0 = mfma(ONES, bf[h][0], cs0);                                                  \
  if (L3) cs1 = mfma(ONES, bf[h][1], cs1);                                          \
  _Pragma("unroll") for (int q = 0; q < 7; ++q) { SGB(0x8, 1); SGB(0x2, 1); SGB(0x100, 1); } \
  SGB(0x8, 5);
  LD_S(0, S, 0)
#pragma nounroll
  for (int s = 0; s < 10; ++s) {
    LAS char* B = S + (s & 3) * STG;
    LAS char* Bn = S + ((s + 1) & 3) * STG;
    LD_S(1, B, 1)
    MM_S(0)
    SCHED();
    BAR();
    SCHED();
    LD_S(0, Bn, 0)
    MM_S(1)
    SCHED();
  }
}

template <bool L3>
__device__ __forceinline__ void loop_ffn(LAS char* S, int xa, const int (&oa)[6], f32x4 (&acc)[6][2], f32x4& cs0, f32x4& cs1, v4u ONES) {
  v4u xf[2][2], wf[2][6];
#define LD_F(h, B, kk)                                                                                                   \
  _Pragma("unroll") for (int f = 0; f < 2; ++f) xf[h][f] = *(volatile LAS v4u*)((B) + ((xa ^ ((kk) * 64)) + f * 2048));  \
  _Pragma("unroll") for (int i = 0; i < 6; ++i) {                                                                        \
    const v2u l2 = __builtin_bit_cast(v2u, __builtin_amdgcn_ds_read_tr16_b64_v4i16((LAS v4s*)((B) + oa[i] + (kk) * 6144)));       \
    const v2u h2 = __builtin_bit_cast(v2u, __builtin_amdgcn_ds_read_tr16_b64_v4i16((LAS v4s*)((B) + oa[i] + (kk) * 6144 + 768))); \
    wf[h][i] = v4u{l2.x, l2.y, h2.x, h2.y};                                                                              \
  }
#define MM_F(h)                                                                     \
  _Pragma("unroll") for (int i = 0; i < 6; ++i) {                                   \
    acc[i][0] = mfma(wf[h][i], xf[h][0], acc[i][0]);                                \
    acc[i][1] = mfma(wf[h][i], xf[h][1], acc[i][1]);                                \
  }                                                                                 \
  if (!L3) { cs0 = mfma(ONES, wf[h][0], cs0); cs1 = mfma(ONES, wf[h][1], cs1); }    \
  _Pragma("unroll") for (int q = 0; q < 7; ++q) { SGB(0x8, 1); SGB(0x2, 1); SGB(0x100, 2); } \
  SGB(0x8, 7);
  LD_F(0, S, 0)
#pragma nounroll
  for (int s = 0; s < 10; ++s) {
    LAS char* B = S + (s & 3) * STG;
    LAS char* Bn = S + ((s + 1) & 3) * STG;
    LD_F(1, B, 1)
    MM_F(0)
    SCHED();
    BAR();
    SCHED();
    LD_F(0, Bn, 0)
    MM_F(1)
    SCHED();
  }
}

extern "C" __global__ __launch_bounds__(768, 3) void kf(const u16* __restrict__ x, const u16* __restrict__ Lre, const u16* __restrict__ Lim,
                                                     const u16* __restrict__ Bre, const u16* __restrict__ Bim, const u16* __restrict__ Cre,
                                                     const u16* __restrict__ Cim, const u16* __restrict__ ldt, const u16* __restrict__ Dsk,
                                                     const u16* __restrict__ w1, const u16* __restrict__ b1, const u16* __restrict__ w2,
                                                     const u16* __restrict__ b2, char* __restrict__ ws, float* __restrict__ out) {
  __shared__ __attribute__((aligned(16))) char smem[K_LDS];
  LAS char* S = (LAS char*)smem;
  const int tid = threadIdx.x, lane = tid & 63, w = __builtin_amdgcn_readfirstlane(tid >> 6);
  const int c = lane & 15, g = lane >> 4;
  const int bid = blockIdx.x, xcd = bid & 7, idx = bid >> 3;
  const int rp = 8 * (xcd & 1) + (idx & 7), cg = 4 * (xcd >> 1) + (idx >> 3);
  const int rt = 8 * (xcd >> 1) + (idx & 7), ct = 4 * (xcd & 1) + (idx >> 3);
  const __amdgpu_buffer_rsrc_t rs = __builtin_amdgcn_make_buffer_rsrc((void*)ws, 0, (int)WS_END, 0x00020000);
  const int rot1 = ((cg + rp) & 3) * 5 / 2;

  if (w < 2) {
    const int e = lane >> 3, chunk = (lane & 7) ^ e;
    const int vb = (e * D_ + 8 * chunk) * 2;
    const __amdgpu_buffer_rsrc_t rbr = __builtin_amdgcn_make_buffer_rsrc((void*)Bre, 0, P_ * D_ * 2, 0x00020000);
    const int sb = 40 * cg * D_ * 2;
    int ks = rot1;
    constexpr int DEEP = 4;
#define P1_SYNC(st) if ((st) >= 2 && (st) < DEEP) { WAITV(19); BAR(); } else if ((st) > DEEP) { WAITV(38); BAR(); }
    if (w == 0) {
      const __amdgpu_buffer_rsrc_t rx = __builtin_amdgcn_make_buffer_rsrc((void*)x, 0, L_ * D_ * 2, 0x00020000);
      const int vx = ((8 * (e >> 2) + (e & 3)) * D_ + 8 * chunk) * 2;
      const int sx = 128 * rp * D_ * 2;
#pragma nounroll
      for (int st = 0; st < 10; ++st) {
        P1_SYNC(st)
        const int bo = (st & 3) * STG, ko = ks * 128;
#pragma unroll
        for (int p = 0; p < 16; ++p)
          DMA16(rx, bo + XOFF + p * 1024, vx, sx + ko + (32 * (p >> 2) + 16 * (p & 1) + 4 * ((p >> 1) & 1)) * D_ * 2, 0);
#pragma unroll
        for (int p = 0; p < 3; ++p) DMA16(rbr, bo + BRE + p * 1024, vb, sb + ko + 8 * p * D_ * 2, 0);
        ks = ks == 9 ? 0 : ks + 1;
      }
    } else {
      const __amdgpu_buffer_rsrc_t rbi = __builtin_amdgcn_make_buffer_rsrc((void*)Bim, 0, P_ * D_ * 2, 0x00020000);
      const __amdgpu_buffer_rsrc_t rw = __builtin_amdgcn_make_buffer_rsrc((void*)w1, 0, D_ * F_ * 2, 0x00020000);
      int vw[3];
#pragma unroll
      for (int b = 0; b < 3; ++b) {
        const int o = 1024 * b + 16 * lane, k = o / 192, bb = o % 192, seg = (bb >> 5) ^ ((k >> 3) & 1);
        vw[b] = (k * F_ + 16 * seg + 8 * ((bb >> 4) & 1)) * 2;
      }
      const int sw = 96 * cg * 2;
#pragma nounroll
      for (int st = 0; st < 10; ++st) {
        P1_SYNC(st)
        const int bo = (st & 3) * STG, ko = ks * 128;
#pragma unroll
        for (int p = 0; p < 12; ++p) DMA16(rw, bo + WOFF + p * 1024, vw[p % 3], sw + (64 * ks + 16 * (p / 3)) * F_ * 2, 0);
#pragma unroll
        for (int p = 3; p < 5; ++p) DMA16(rbr, bo + BRE + p * 1024, vb, sb + ko + 8 * p * D_ * 2, 0);
#pragma unroll
        for (int p = 0; p < 5; ++p) DMA16(rbi, bo + BIM + p * 1024, vb, sb + ko + 8 * p * D_ * 2, 0);
        ks = ks == 9 ? 0 : ks + 1;
      }
    }
    const __amdgpu_buffer_rsrc_t rcr = __builtin_amdgcn_make_buffer_rsrc((void*)Cre, 0, D_ * P_ * 2, 0x00020000);
    const __amdgpu_buffer_rsrc_t rci = __builtin_amdgcn_make_buffer_rsrc((void*)Cim, 0, D_ * P_ * 2, 0x00020000);
    int dst = 8 * 10240 + w * 5120;
#define NEXT_U() { dst += 10240; if (dst >= 14 * 10240) dst -= 14 * 10240; }
#define ISSUE_W(j) { _Pragma("unroll") for (int p = 0; p < 5; ++p) DMA16(rs, dst + p * 1024, lane * 16, WS_W2T + (ct * 48 + 2 * (j) + w) * 5120 + p * 1024, 16); NEXT_U() }
#define ISSUE_C(rc, m) { _Pragma("unroll") for (int p = 0; p < 5; ++p) DMA16(rc, dst + p * 1024, vb, (80 * ct + 40 * w + 8 * p) * P_ * 2 + (m) * 128, 0); NEXT_U() }
    WAITV(38); BAR();
    WAITV(19); BAR();
    WAITV(0); BAR();
#pragma nounroll
    for (int j = 0; j < PFA; ++j) ISSUE_W(j)
    BAR();
#pragma nounroll
    for (int j = PFA; j < PFB; ++j) ISSUE_W(j)
    BAR();
#pragma nounroll
    for (int j = PFB; j < DB; ++j) ISSUE_W(j)
#pragma nounroll
    for (int j = DB; j < 24; j += 2) { WAITV(50); BAR(); ISSUE_W(j) ISSUE_W(j + 1) }
#pragma nounroll
    for (int m = 0; m < 10; ++m) { WAITV(50); BAR(); ISSUE_C(rcr, m) ISSUE_C(rci, m) }
    WAITV(50); BAR(); WAITV(40); BAR(); WAITV(30); BAR(); WAITV(20); BAR(); WAITV(10); BAR(); WAITV(0); BAR();
    return;
  }

  if (w < 4) {
    __builtin_amdgcn_s_sleep(8);
    {
      const __amdgpu_buffer_rsrc_t rw2 = __builtin_amdgcn_make_buffer_rsrc((void*)w2, 0, F_ * D_ * 2, 0x00020000);
      u16 pe[4][8];
      const int bw0 = rt < 30 ? 8 * rt + 4 * (w - 2) : 0;
#pragma unroll
      for (int i = 0; i < 4; ++i) {
        const int b = bw0 + i, so = (32 * (b / 5) * D_ + 80 * ct + 16 * (b % 5)) * 2;
#pragma unroll
        for (int j = 0; j < 8; ++j)
          pe[i][j] = __builtin_amdgcn_raw_buffer_load_b16(rw2, (4 * g * D_ + c) * 2 + (j & 3) * D_ * 2, so + 16 * (j >> 2) * D_ * 2, 0);
      }
      const unsigned ow = rt < 30 ? WS_W2T + (ct * 240 + bw0) * 1024 + lane * 16 : 0x40000000u;
#pragma unroll
      for (int i = 0; i < 4; ++i) {
        SCHED(); BAR(); SCHED();
        const v4u o = {pe[i][0] | ((unsigned)pe[i][1] << 16), pe[i][2] | ((unsigned)pe[i][3] << 16), pe[i][4] | ((unsigned)pe[i][5] << 16), pe[i][6] | ((unsigned)pe[i][7] << 16)};
        __builtin_amdgcn_raw_buffer_store_b128(o, rs, ow + i * 1024, 0, 16);
      }
    }
    BAR();
    WAITV(0);
    BAR();
    if (w == 2) {
      const u64 T = *(LAS u64*)(S + SY_T);
      if (lane == 0) __hip_atomic_store((gu64*)(ws + WS_PT) + ct * 32 + rt, T, RLX_AGENT);
      BAR(); BAR();
      gu64* pp = (gu64*)(ws + WS_PT) + ct * 32 + (lane & 31);
      const u64 pv = __hip_atomic_load(pp, RLX_AGENT);
      BAR();
      poll_flags(pp, pv, T);
      {
        unsigned t[2];
#pragma unroll
        for (int i = 0; i < 2; ++i)
          t[i] = __builtin_amdgcn_raw_buffer_load_b32(rs, (i < 1 || lane < 56 ? lane : 0) * 128, WS_W2T + (ct * 240 + 120 + (rt & 7) * 15) * 1024 + i * 8192, 16);
        SCHED();
        BAR(); BAR();
        SCHED();
#pragma unroll
        for (int i = 0; i < 2; ++i) asm volatile("" :: "v"(t[i]));
      }
      BAR();
      __builtin_amdgcn_s_sleep(FH_DELAY);
      slow_flags<FH_SLP>((gu64*)(ws + WS_FH) + rt * 32 + (lane & 31), T);
      lds_set(S, SY_HOK, 1u, lane);
    } else {
      const u64 T = *(LAS u64*)(S + SY_T);
      bool done = false;
      const __amdgpu_buffer_rsrc_t rc = __builtin_amdgcn_make_buffer_rsrc((void*)((rt & 2) ? Cim : Cre), 0, D_ * P_ * 2, 0x00020000);
#pragma nounroll
      for (int ksp = 5; ksp < 10 + (int)SPIN_CAP; ++ksp) {
        v4u g0 = {0, 0, 0, 0}, g1 = {0, 0, 0, 0};
        asm volatile("" ::: "memory");
        if (!done) {
          g0 = __builtin_amdgcn_raw_buffer_load_b128(rs, WS_STAT + (128 * rp + lane) * 16, 0, 16);
          g1 = __builtin_amdgcn_raw_buffer_load_b128(rs, WS_STAT + (128 * rp + 64 + lane) * 16, 0, 16);
        }
        unsigned u[2];
        if (ksp < 9) {
#pragma unroll
          for (int i = 0; i < 1; ++i) {
            const int ln = (ksp - 5) * 64 + lane;
            u[i] = __builtin_amdgcn_raw_buffer_load_b32(rc, (ln < 200 ? ln : 0) * 128, 80 * ct * P_ * 2 + ((rt & 1) * 2 + ((rt >> 2) & 1)) * 25600, 0);
          }
          SCHED();
          BAR();
          SCHED();
#pragma unroll
          for (int i = 0; i < 1; ++i) asm volatile("" :: "v"(u[i]));
        }
        if (!done && __all(g0.z == (unsigned)T && g0.w == (unsigned)(T >> 32) && g1.z == (unsigned)T && g1.w == (unsigned)(T >> 32))) {
          const int lr = (lane & 32) + 16 * ((lane >> 2) & 1) + 4 * ((lane >> 3) & 3) + (lane & 3);
          *(LAS unsigned*)(S + M_MRS + lr * 4) = g0.x; *(LAS unsigned*)(S + M_RS + lr * 4) = g0.y;
          *(LAS unsigned*)(S + M_MRS + (64 + lr) * 4) = g1.x; *(LAS unsigned*)(S + M_RS + (64 + lr) * 4) = g1.y;
          done = true;
        }
        if (done && ksp >= 8) break;
      }
      BAR();
      BAR();
#pragma nounroll
      for (int i = 0; i < FS_B0; ++i) BAR();
      gu64* fp = (gu64*)(ws + WS_FS) + rt * 32 + (lane & 31);
      const u64 fv = __hip_atomic_load(fp, RLX_AGENT);
#pragma nounroll
      for (int i = FS_B0; i < 12 - DA / 2; ++i) BAR();
      poll_flags(fp, fv, T);
      BAR();
    }
    return;
  }

  const int role = (w - 4) >> 2, mw = w & 3;
  LAS float* CS = (LAS float*)(S + M_CS);
  cpx wt[3] = {{0.f, 0.f}, {0.f, 0.f}, {0.f, 0.f}};
  if (role == 0) {
    const int q = 10 * mw + (lane < 10 ? lane : 9), ch = 40 * cg + q;
    const float lr = bf2f(Lre[ch]), li = bf2f(Lim[ch]);
    const float dt = rbf(expf(bf2f(ldt[ch])));
    const float e = expf(dt * lr);
    float sn, cn; sincosf(dt * li, &sn, &cn);
    const cpx a1 = {e * cn, e * sn};
    const float nr = a1.r - 1.f, den = 1.f / (lr * lr + li * li);
    const float cr = (nr * lr + a1.i * li) * den, ci = (a1.i * lr - nr * li) * den;
    const cpx a8 = csq(csq(csq(a1))), a16 = csq(a8), a32 = csq(a16), a24 = cmul(a8, a16);
    if (lane < 10) {
#pragma unroll
      for (int comp = 0; comp < 2; ++comp) {
        const float sg = comp ? 1.f : -1.f;
        LAS f32x4* t = (LAS f32x4*)(S + M_CT + (2 * q + comp) * 48);
        t[0] = f32x4{a1.r, sg * a1.i, comp ? -cr : cr, -ci};
        t[1] = f32x4{a8.r, sg * a8.i, a16.r, sg * a16.i};
        t[2] = f32x4{a24.r, sg * a24.i, a32.r, sg * a32.i};
      }
    }
    const cpx a128 = csq(csq(a32));
    const int ex = c < rp ? rp - 1 - c : 0;
#pragma unroll
    for (int r = 0; r < 3; ++r) {
      cpx A = {bperm((4 * r + g) * 4, a128.r), bperm((4 * r + g) * 4, a128.i)};
      cpx m = (ex & 1) ? A : cpx{1.f, 0.f};
      A = csq(A); { const cpx t = cmul(m, A); m = (ex & 2) ? t : m; }
      A = csq(A); { const cpx t = cmul(m, A); m = (ex & 4) ? t : m; }
      A = csq(A); { const cpx t = cmul(m, A); m = (ex & 8) ? t : m; }
      wt[r] = c < rp ? m : cpx{0.f, 0.f};
      asm volatile("" : "+v"(wt[r].r), "+v"(wt[r].i));
    }
  } else {
    const u64 T = __hip_atomic_load((gu64*)(ws + WS_E), RLX_AGENT) + 1ull;
    const __amdgpu_buffer_rsrc_t rx = __builtin_amdgcn_make_buffer_rsrc((void*)x, 0, L_ * D_ * 2, 0x00020000);
    const int row = 128 * rp + 8 * cg + 2 * mw + (lane >> 5), l5 = lane & 31;
    v4u xr[3];
    xr[0] = __builtin_amdgcn_raw_buffer_load_b128(rx, row * D_ * 2 + l5 * 16, 0, 0);
    xr[1] = __builtin_amdgcn_raw_buffer_load_b128(rx, row * D_ * 2 + l5 * 16, 512, 0);
    xr[2] = __builtin_amdgcn_raw_buffer_load_b128(rx, row * D_ * 2 + (l5 & 15) * 16, 1024, 0);
    if (w == 8 && lane < 48) *(LAS unsigned*)(S + M_B1 + 4 * lane) = *(const GAS unsigned*)(b1 + 96 * cg + 2 * lane);
    if (w == 9 && lane == 0) {
      *(LAS u64*)(S + SY_T) = T;
      *(LAS v4u*)(S + SY_C1) = v4u{0, 0, 0x3f800000u, 0};
#pragma unroll
      for (int i = 1; i < 5; ++i) *(LAS v4u*)(S + M_SYNC + 16 * i) = v4u{0, 0, 0, 0};
    }
    const float wl = l5 < 16 ? 1.f : 0.f;
    float s0 = 0.f, s2 = 0.f, u_, v_;
#pragma unroll
    for (int e = 0; e < 4; ++e) {
      s0 += bflo(xr[0][e]) + bfhi(xr[0][e]); s0 += bflo(xr[1][e]) + bfhi(xr[1][e]); s2 += bflo(xr[2][e]) + bfhi(xr[2][e]);
    }
    float sm_ = fmaf(s2, wl, s0);
    sm_ += dpp<0x128>(sm_); sm_ += dpp<0x124>(sm_); sm_ += dpp<0x122>(sm_); sm_ += dpp<0x121>(sm_);
    rows16(sm_, u_, v_); sm_ = u_ + v_;
    const float m = sm_ * (1.f / D_);
    float q0 = 0.f, q2 = 0.f;
#pragma unroll
    for (int e = 0; e < 4; ++e) {
      float d;
      d = bflo(xr[0][e]) - m; q0 = fmaf(d, d, q0); d = bfhi(xr[0][e]) - m; q0 = fmaf(d, d, q0);
      d = bflo(xr[1][e]) - m; q0 = fmaf(d, d, q0); d = bfhi(xr[1][e]) - m; q0 = fmaf(d, d, q0);
      d = bflo(xr[2][e]) - m; q2 = fmaf(d, d, q2); d = bfhi(xr[2][e]) - m; q2 = fmaf(d, d, q2);
    }
    float q = fmaf(q2, wl, q0);
    q += dpp<0x128>(q); q += dpp<0x124>(q); q += dpp<0x122>(q); q += dpp<0x121>(q);
    rows16(q, u_, v_); q = u_ + v_;
    const float mb = rbf(m), sd = rbf(__builtin_amdgcn_sqrtf(rbf(rbf(q * (1.f / D_)) + 1e-5f)));
    const float ri = __builtin_amdgcn_rcpf(sd);
    if (l5 == 0)
      __builtin_amdgcn_raw_buffer_store_b128(v4u{asu(-mb * ri), asu(ri), (unsigned)T, (unsigned)(T >> 32)}, rs, WS_STAT + row * 16, 0, 16);
  }
  const int xa = XOFF + (32 * mw + c) * 128 + ((g ^ (c & 7)) << 4);
  f32x4 cs0 = {0, 0, 0, 0}, cs1 = {0, 0, 0, 0};
  v4u ONES = {0x3f803f80u, 0x3f803f80u, 0x3f803f80u, 0x3f803f80u};
  asm volatile("" : "+v"(ONES));
  BAR();
  const unsigned fslot = (2 * rp + (mw >> 1)) * 32 + cg * 2 + (mw & 1);
  const int tok = 64 * rt + 32 * (mw >> 1) + 8 * (c >> 2) + 4 * (mw & 1) + (c & 3);

  if (role == 0) {
    int oa[5], col[5];
#pragma unroll
    for (int i = 0; i < 5; ++i) {
      int j = i + mw; j = j >= 5 ? j - 5 : j;
      col[i] = 16 * j + c;
      oa[i] = ((c & 1) ? BIM : BRE) + (8 * j + (c >> 1)) * 128 + ((g ^ (c >> 1)) << 4);
    }
    f32x4 acc[5][2];
#pragma unroll
    for (int i = 0; i < 5; ++i) { acc[i][0] = f32x4{0, 0, 0, 0}; acc[i][1] = f32x4{0, 0, 0, 0}; }
    if (mw == 3) loop_ssm<true>(S, xa, oa, acc, cs0, cs1, ONES);
    else loop_ssm<false>(S, xa, oa, acc, cs0, cs1, ONES);
    __builtin_amdgcn_s_setprio(2);
    if (g == 0) {
      CS[16 * mw + c] = cs0[0];
      if (mw == 3) CS[64 + c] = cs1[0];
    }
    f32x4 k0[5], k1_[5];
#pragma unroll
    for (int i = 0; i < 5; ++i) {
      k0[i] = *(LAS f32x4*)(S + M_CT + col[i] * 48);
      k1_[i] = *(LAS f32x4*)(S + M_CT + col[i] * 48 + 16);
    }
    const u64 T = *(LAS u64*)(S + SY_T);
    BAR();
    float v[5][8], E[5];
    {
      f32x4 mt4[2], rt4[2];
      float cs[5];
#pragma unroll
      for (int f = 0; f < 2; ++f) {
        mt4[f] = *(LAS f32x4*)(S + M_MRS + (32 * mw + 16 * f + 4 * g) * 4);
        rt4[f] = *(LAS f32x4*)(S + M_RS + (32 * mw + 16 * f + 4 * g) * 4);
      }
#pragma unroll
      for (int i = 0; i < 5; ++i) cs[i] = CS[col[i]];
#pragma unroll
      for (int f = 0; f < 2; ++f)
#pragma unroll
        for (int i = 0; i < 5; ++i) {
          const f32x4 t = acc[i][f] * rt4[f] + mt4[f] * cs[i];
#pragma unroll
          for (int r = 0; r < 4; ++r) v[i][4 * f + r] = t[r];
        }
      float ar[5], ai[5];
#pragma unroll
      for (int i = 0; i < 5; ++i) { ar[i] = k0[i].x; ai[i] = k0[i].y; }
#pragma unroll
      for (int s = 1; s < 8; ++s)
        asm volatile("v_fmac_f32 %0, %10, %5\n\tv_fmac_f32 %1, %11, %6\n\tv_fmac_f32 %2, %12, %7\n\tv_fmac_f32 %3, %13, %8\n\tv_fmac_f32 %4, %14, %9\n\t"
                     "v_fmac_f32_dpp %0, %5, %15 quad_perm:[1,0,3,2] row_mask:0xf bank_mask:0xf\n\t"
                     "v_fmac_f32_dpp %1, %6, %16 quad_perm:[1,0,3,2] row_mask:0xf bank_mask:0xf\n\t"
                     "v_fmac_f32_dpp %2, %7, %17 quad_perm:[1,0,3,2] row_mask:0xf bank_mask:0xf\n\t"
                     "v_fmac_f32_dpp %3, %8, %18 quad_perm:[1,0,3,2] row_mask:0xf bank_mask:0xf\n\t"
                     "v_fmac_f32_dpp %4, %9, %19 quad_perm:[1,0,3,2] row_mask:0xf bank_mask:0xf\n\ts_nop 1"
                     : "+v"(v[0][s]), "+v"(v[1][s]), "+v"(v[2][s]), "+v"(v[3][s]), "+v"(v[4][s])
                     : "v"(v[0][s - 1]), "v"(v[1][s - 1]), "v"(v[2][s - 1]), "v"(v[3][s - 1]), "v"(v[4][s - 1]),
                       "v"(ar[0]), "v"(ar[1]), "v"(ar[2]), "v"(ar[3]), "v"(ar[4]), "v"(ai[0]), "v"(ai[1]), "v"(ai[2]), "v"(ai[3]), "v"(ai[4]));
      float r0[5], q0[5], W[5];
#pragma unroll
      for (int i = 0; i < 5; ++i) {
        float r1, q1;
        rows16(v[i][7], r0[i], r1);
        const float Pp = r1 + cm(k1_[i].x, k1_[i].y, r0[i]);
        rows32(Pp, q0[i], q1);
        W[i] = q1 + cm(k1_[i].z, k1_[i].w, q0[i]);
      }
      if (g == 0) {
#pragma unroll
        for (int i = 0; i < 5; ++i) *(LAS float*)(S + M_WAGG + (mw * 80 + col[i]) * 4) = W[i];
      }
      WAITL();
      unsigned old = 0;
      if (lane == 0) old = __hip_atomic_fetch_add((LAS unsigned*)(S + SY_C1), 1u, RLX_WG);
#pragma unroll
      for (int i = 0; i < 5; ++i) {
        const float X = r0[i] + cm(k1_[i].x, k1_[i].y, q0[i]);
        const float lo = (g & 1) ? r0[i] : 0.f, hi = (g & 1) ? X : q0[i];
        E[i] = (g & 2) ? hi : lo;
      }
      old = __builtin_amdgcn_readfirstlane(old);
      if (old == 3) {
        asm volatile("" ::: "memory");
        const int q = lane < 40 ? lane : 39;
        const f32x2 k2 = *(LAS f32x2*)(S + M_CT + (2 * q + 1) * 48 + 40);
        f32x2 wv[4];
#pragma unroll
        for (int ww = 0; ww < 4; ++ww) wv[ww] = *(LAS f32x2*)(S + M_WAGG + (ww * 80 + 2 * q) * 4);
        const cpx a32 = {k2.x, k2.y};
        cpx ag = {wv[0].x, wv[0].y};
#pragma unroll
        for (int ww = 1; ww < 4; ++ww) { ag = cmul(ag, a32); ag.r += wv[ww].x; ag.i += wv[ww].y; }
        if (lane < 40)
          __builtin_amdgcn_raw_buffer_store_b128(v4u{asu(ag.r), asu(ag.i), (unsigned)T, (unsigned)(T >> 32)}, rs,
                                                 WS_AGG + ((cg * 40 + q) * 16 + rp) * 16, 0, 16);
      }
    }
#pragma unroll
    for (int s = 0; s < 8; ++s)
#pragma unroll
      for (int i = 0; i < 5; ++i) v[i][s] = cm(k0[i].z, k0[i].w, v[i][s]);
#pragma unroll
    for (int i = 0; i < 5; ++i)
      asm volatile("" : "+v"(v[i][0]), "+v"(v[i][1]), "+v"(v[i][2]), "+v"(v[i][3]), "+v"(v[i][4]), "+v"(v[i][5]), "+v"(v[i][6]), "+v"(v[i][7]));
    f32x2 a32[5], ag8[5];
#pragma unroll
    for (int i = 0; i < 5; ++i) {
      a32[i] = *(LAS f32x2*)(S + M_CT + col[i] * 48 + 40);
      ag8[i] = *(LAS f32x2*)(S + (g ? M_CT + col[i] * 48 + 8 + 8 * g : SY_ONE));
    }
    {
      int chn[3];
      const bool act = c < rp;
#pragma unroll
      for (int r = 0; r < 3; ++r) chn[r] = 10 * mw + (4 * r + g < 10 ? 4 * r + g : 9);
      v4u gr[3] = {{0, 0, 0, 0}, {0, 0, 0, 0}, {0, 0, 0, 0}};
      if (rp > 0) {
        gu64* tp = (gu64*)(ws + WS_AGG + ((cg * 40 + 10 * mw) * 16 + (act ? c : 0)) * 16 + 8);
#pragma nounroll
        for (unsigned sp = 0; sp < SPIN_CAP; ++sp) {
          if (__all(__hip_atomic_load(tp, RLX_AGENT) == T)) break;
          __builtin_amdgcn_s_sleep(1);
        }
#pragma nounroll
        for (unsigned sp = 0; sp < SPIN_CAP; ++sp) {
          asm volatile("" ::: "memory");
          bool ok = true;
#pragma unroll
          for (int r = 0; r < 3; ++r) gr[r] = __builtin_amdgcn_raw_buffer_load_b128(rs, WS_AGG + ((cg * 40 + chn[r]) * 16 + c) * 16, 0, 16);
#pragma unroll
          for (int r = 0; r < 3; ++r) ok = ok && gr[r].z == (unsigned)T && gr[r].w == (unsigned)(T >> 32);
          if (__all(ok || !act)) break;
        }
      }
      cpx h[3];
#pragma unroll
      for (int r = 0; r < 3; ++r) {
        const cpx ag = {act ? asf(gr[r].x) : 0.f, act ? asf(gr[r].y) : 0.f};
        h[r] = cmul(wt[r], ag);
      }
#pragma unroll
      for (int r = 0; r < 3; ++r) { h[r].r += dpp<0x128>(h[r].r); h[r].i += dpp<0x128>(h[r].i); }
#pragma unroll
      for (int r = 0; r < 3; ++r) { h[r].r += dpp<0x124>(h[r].r); h[r].i += dpp<0x124>(h[r].i); }
#pragma unroll
      for (int r = 0; r < 3; ++r) { h[r].r += dpp<0x122>(h[r].r); h[r].i += dpp<0x122>(h[r].i); }
#pragma unroll
      for (int r = 0; r < 3; ++r) { h[r].r += dpp<0x121>(h[r].r); h[r].i += dpp<0x121>(h[r].i); }
      if (c == 0) {
#pragma unroll
        for (int r = 0; r < 3; ++r) *(LAS f32x2*)(S + M_HIN + chn[r] * 8) = f32x2{h[r].r, h[r].i};
      }
      WAITL();
      if (lane == 0) __hip_atomic_fetch_add((LAS unsigned*)(S + SY_C2), 1u, RLX_WG);
      lds_wait_ge(S, SY_C2, 4u);
    }
    {
      float cw[5];
#pragma unroll
      for (int i = 0; i < 5; ++i) cw[i] = *(LAS float*)(S + M_HIN + col[i] * 4);
      float wa[3][5];
#pragma unroll
      for (int ww = 0; ww < 3; ++ww)
#pragma unroll
        for (int i = 0; i < 5; ++i) wa[ww][i] = *(LAS float*)(S + M_WAGG + (ww * 80 + col[i]) * 4);
#pragma unroll
      for (int ww = 0; ww < 3; ++ww) {
        if (ww < mw) {
#pragma unroll
          for (int i = 0; i < 5; ++i) cw[i] = cm(a32[i].x, a32[i].y, cw[i]) + wa[ww][i];
        }
      }
#pragma unroll
      for (int i = 0; i < 5; ++i) cw[i] = cm(ag8[i].x, ag8[i].y, cw[i]) + E[i];
#pragma unroll
      for (int i = 0; i < 5; ++i) cw[i] = cm(k0[i].z, k0[i].w, cw[i]);
#pragma unroll
      for (int s = 0; s < 8; ++s)
#pragma unroll
        for (int i = 0; i < 5; ++i) { cw[i] = cm(k0[i].x, -k0[i].y, cw[i]); v[i][s] += cw[i]; }
      LAS char* IMG = S + M_IMG + mw * 3072;
      LAS char* wa_ = IMG + c * 64 + ((g ^ ((c >> 1) & 3)) << 4);
      const int q = c >> 2, p = c & 3, f = g >> 1, comp = g & 1;
      LAS char* ra_ = IMG + (2 * q + comp) * 64 + ((p ^ q) << 4) + 8 * f;
      const unsigned ob = WS_HS + ((2 * rp + (mw >> 1)) * 160 + comp * 8 + 2 * (mw & 1) + f) * 1024 + c * 16;
#pragma unroll
      for (int h = 0; h < 2; ++h) {
#pragma unroll
        for (int i = 3 * h; i < (h ? 5 : 3); ++i)
          *(LAS v4u*)(wa_ + (i - 3 * h) * 1024) = v4u{pk2(v[i][0], v[i][1]), pk2(v[i][2], v[i][3]), pk2(v[i][4], v[i][5]), pk2(v[i][6], v[i][7])};
#pragma unroll
        for (int i = 3 * h; i < (h ? 5 : 3); ++i) {
          const v2u lo = __builtin_bit_cast(v2u, __builtin_amdgcn_ds_read_tr16_b64_v4i16((LAS v4s*)(ra_ + (i - 3 * h) * 1024)));
          const v2u hi = __builtin_bit_cast(v2u, __builtin_amdgcn_ds_read_tr16_b64_v4i16((LAS v4s*)(ra_ + (i - 3 * h) * 1024 + 512)));
          int j = i + mw; j = j >= 5 ? j - 5 : j;
          const int GO = 5 * cg + j;
          __builtin_amdgcn_raw_buffer_store_b128(v4u{lo.x, lo.y, hi.x, hi.y}, rs, ob, ((GO >> 3) * 16 + ((GO >> 2) & 1) * 4) * 1024 + (GO & 3) * 256, 16);
        }
      }
    }
    __builtin_amdgcn_s_setprio(0);
  } else {
    const int rot = mw < 2 ? mw : 2;
    int oa[6];
#pragma unroll
    for (int i = 0; i < 6; ++i) {
      int pp = (i >> 1) + rot; pp = pp >= 3 ? pp - 3 : pp;
      oa[i] = WOFF + (8 * g + (c >> 2)) * 192 + 64 * pp + 32 * ((i & 1) ^ (g & 1)) + 8 * (c & 3);
    }
    f32x4 acc[6][2];
#pragma unroll
    for (int i = 0; i < 6; ++i) { acc[i][0] = f32x4{0, 0, 0, 0}; acc[i][1] = f32x4{0, 0, 0, 0}; }
    if (mw == 3) loop_ffn<true>(S, xa, oa, acc, cs0, cs1, ONES);
    else loop_ffn<false>(S, xa, oa, acc, cs0, cs1, ONES);
    if (g == 0 && mw != 3) {
      CS[80 + 32 * rot + c] = cs0[0];
      CS[80 + 32 * rot + 16 + c] = cs1[0];
    }
    BAR();
    float mrs[2], rsd[2];
#pragma unroll
    for (int f = 0; f < 2; ++f) {
      mrs[f] = *(LAS float*)(S + M_MRS + (32 * mw + 16 * f + c) * 4);
      rsd[f] = *(LAS float*)(S + M_RS + (32 * mw + 16 * f + c) * 4);
    }
#pragma unroll
    for (int pp = 0; pp < 3; ++pp) {
      int pq = pp + rot; pq = pq >= 3 ? pq - 3 : pq;
      unsigned o[2][4];
#pragma unroll
      for (int e = 0; e < 2; ++e) {
        const f32x4 cs = *(LAS f32x4*)(S + M_CS + (80 + 32 * pq + 16 * e + 4 * g) * 4);
        const v2u bb = *(LAS v2u*)(S + M_B1 + (32 * pq + 16 * e + 4 * g) * 2);
        const f32x4 bv = {bflo(bb.x), bfhi(bb.x), bflo(bb.y), bfhi(bb.y)};
#pragma unroll
        for (int f = 0; f < 2; ++f) {
          const f32x4 t = acc[2 * pp + e][f] * rsd[f] + (cs * mrs[f] + bv);
          const f32x4 z = t * (t * t * (0.044715f * -2.302208198f) + -2.302208198f);
          f32x4 d;
#pragma unroll
          for (int r = 0; r < 4; ++r) d[r] = __builtin_amdgcn_exp2f(z[r]);
          d = d + 1.f;
#pragma unroll
          for (int r = 0; r < 4; ++r) d[r] = __builtin_amdgcn_rcpf(d[r]);
          const f32x4 hv = t * d;
          o[f][2 * e] = pk2(hv[0], hv[1]); o[f][2 * e + 1] = pk2(hv[2], hv[3]);
        }
      }
#pragma unroll
      for (int f = 0; f < 2; ++f)
        __builtin_amdgcn_raw_buffer_store_b128(v4u{o[f][0], o[f][1], o[f][2], o[f][3]}, rs,
                                               WS_HF + (((2 * rp + (mw >> 1)) * 48 + 3 * cg + pq) * 4 + 2 * (mw & 1) + f) * 1024 + lane * 16, 0, 16);
    }
    WAITV(0);
    const u64 T = *(LAS u64*)(S + SY_T);
    if (lane == 0) __hip_atomic_store((gu64*)(ws + WS_FH) + fslot, T, RLX_AGENT);
    if (bid == 0 && w == 8) {
      gu64* rd = (gu64*)(ws + WS_PT) + lane;
#pragma nounroll
      for (unsigned sp = 0; sp < SPIN_CAP; ++sp) {
        const u64 a = __hip_atomic_load(rd, RLX_AGENT), b = __hip_atomic_load(rd + 64, RLX_AGENT),
                  cc = __hip_atomic_load(rd + 128, RLX_AGENT), d = __hip_atomic_load(rd + 192, RLX_AGENT);
        if (__all(a == T && b == T && cc == T && d == T)) break;
        __builtin_amdgcn_s_sleep(4);
      }
      if (lane == 0) __hip_atomic_store((gu64*)(ws + WS_E), T, RLX_AGENT);
    }
  }
  lds_wait_sl(S, SY_HOK, 1u);

  f32x4 accF[5], accS[5];
#pragma unroll
  for (int n = 0; n < 5; ++n) { accF[n] = f32x4{0, 0, 0, 0}; accS[n] = f32x4{0, 0, 0, 0}; }
  v2u xv[3], dv[3], bv[3];
  v4u sg;
  {
    const u64 Tq = *(LAS u64*)(S + SY_T);
    const int av = (role * 4 + mw) * 1024 + lane * 16;
    const int aF = WS_HF + rt * 48 * 4096, aS = WS_HS + rt * 40 * 4096 - 24 * 8192;
    const int blF = role * 5120 + lane * 16, blS = c * 128 + (((4 * role + g) ^ (c & 7)) << 4);
    v4u a[DA], b[4][5];
#define LDA2(j) a[(j) % DA] = __builtin_amdgcn_raw_buffer_load_b128(rs, av, ((j) < 24 ? aF : aS) + (j) * 8192, 16)
#pragma unroll
    for (int j = 0; j < DA; ++j) LDA2(j);
    if (role == 0) {
      asm volatile("s_waitcnt vmcnt(%0)" :: "i"(DA) : "memory");
      if (lane == 0) __hip_atomic_store((gu64*)(ws + WS_FS) + fslot, Tq, RLX_AGENT);
    }
#pragma unroll
    for (int J = 0; J <= NU / 2; ++J) {
      if (J < NU / 2) {
        BAR();
        SCHED();
#pragma unroll
        for (int u = 0; u < 2; ++u) {
          const int j = 2 * J + u;
          int bp = (j < 24 ? blF : blS) + slotu(j);
          asm volatile("" : "+v"(bp));
#pragma unroll
          for (int n = 0; n < 5; ++n) b[j & 3][n] = *(volatile LAS v4u*)(S + bp + n * (j < 24 ? 1024 : 2048));
        }
      }
      if (J > 0) {
#pragma unroll
        for (int u = 0; u < 2; ++u) {
          const int j = 2 * J - 2 + u;
#pragma unroll
          for (int n = 0; n < 5; ++n) {
            if (j < 24) accF[n] = mfma(b[j & 3][n], a[j % DA], accF[n]);
            else accS[n] = mfma(b[j & 3][n], a[j % DA], accS[n]);
          }
          if (j + DA < NU) LDA2(j + DA);
        }
        if (J < NU / 2) {
          SGB(0x2, 2);
#pragma unroll
          for (int q = 0; q < 5; ++q) { SGB(0x8, 1); SGB(0x100, 2); }
          SGB(0x20, 1); SGB(0x8, 5); SGB(0x20, 1);
        }
      }
      if (J == NU / 2 - 2) {
#pragma unroll
        for (int n = 0; n < 3; ++n) {
          const int nn = role ? (n < 2 ? 3 + n : 4) : n, d = 80 * ct + 16 * nn + 4 * g;
          xv[n] = *(const GAS v2u*)(x + tok * D_ + d);
          dv[n] = *(const GAS v2u*)(Dsk + d);
          bv[n] = *(const GAS v2u*)(b2 + d);
        }
        sg = __builtin_amdgcn_raw_buffer_load_b128(rs, WS_STAT + tok * 16, 0, 16);
      }
      SCHED();
    }
  }
  {
    LAS char* Xc = S + lane * 16;
    if (role == 0) {
#pragma unroll
      for (int n = 3; n < 5; ++n) {
        *(LAS f32x4*)(Xc + (mw * 10 + n) * 1024) = accF[n];
        *(LAS f32x4*)(Xc + (mw * 10 + 5 + n) * 1024) = accS[n];
      }
    } else {
#pragma unroll
      for (int n = 0; n < 3; ++n) {
        *(LAS f32x4*)(Xc + (mw * 10 + n) * 1024) = accF[n];
        *(LAS f32x4*)(Xc + (mw * 10 + 5 + n) * 1024) = accS[n];
      }
      accF[0] = accF[3]; accF[1] = accF[4]; accF[2] = accF[4];
      accS[0] = accS[3]; accS[1] = accS[4]; accS[2] = accS[4];
    }
    {
      const u64 T = *(LAS u64*)(S + SY_T);
#pragma nounroll
      for (unsigned sp = 0; sp < SPIN_CAP && !allw(sg.z == (unsigned)T && sg.w == (unsigned)(T >> 32)); ++sp) {
        asm volatile("" ::: "memory");
        sg = __builtin_amdgcn_raw_buffer_load_b128(rs, WS_STAT + tok * 16, 0, 16);
      }
    }
    BAR();
    const float mrs = asf(sg.x), rsd = asf(sg.y);
    const __amdgpu_buffer_rsrc_t ro = __builtin_amdgcn_make_buffer_rsrc((void*)out, 0, L_ * D_ * 4, 0x00020000);
#pragma unroll
    for (int n = 0; n < 3; ++n) {
      const int nn = role ? (n < 2 ? 3 + n : 4) : n;
      const f32x4 fs = accF[n] + *(LAS f32x4*)(Xc + (mw * 10 + nn) * 1024);
      const f32x4 ss = accS[n] + *(LAS f32x4*)(Xc + (mw * 10 + 5 + nn) * 1024);
      const f32x4 xs = {bflo(xv[n].x), bfhi(xv[n].x), bflo(xv[n].y), bfhi(xv[n].y)};
      const f32x4 ds = {bflo(dv[n].x), bfhi(dv[n].x), bflo(dv[n].y), bfhi(dv[n].y)};
      const f32x4 bs = {bflo(bv[n].x), bfhi(bv[n].x), bflo(bv[n].y), bfhi(bv[n].y)};
      const f32x4 vv = fs + bs;
      f32x4 d = vv * -1.442695041f;
#pragma unroll
      for (int r = 0; r < 4; ++r) d[r] = __builtin_amdgcn_exp2f(d[r]);
      d = d + 1.f;
#pragma unroll
      for (int r = 0; r < 4; ++r) d[r] = __builtin_amdgcn_rcpf(d[r]);
      const f32x4 gg = vv * d;
      const f32x4 o = ((xs * rsd + mrs) * ds + ss * 2.f) * gg + xs;
      if (role == 0 || n < 2) __builtin_amdgcn_raw_buffer_store_b128(__builtin_bit_cast(v4u, o), ro, (tok * D_ + 80 * ct + 16 * nn + 4 * g) * 4, 0, OUT_AUX);
    }
  }
}

extern "C" void kernel_launch(void* const* d_in, const int* in_sizes, int n_in, void* d_out, int out_size, void* d_ws, size_t ws_size,
                              hipStream_t stream) {
  kf<<<256, 768, 0, stream>>>((const u16*)d_in[0], (const u16*)d_in[1], (const u16*)d_in[2], (const u16*)d_in[3], (const u16*)d_in[4],
                              (const u16*)d_in[5], (const u16*)d_in[6], (const u16*)d_in[7], (const u16*)d_in[8], (const u16*)d_in[10],
                              (const u16*)d_in[11], (const u16*)d_in[12], (const u16*)d_in[13], (char*)d_ws, (float*)d_out);
}
```
